```python
import torch

def _softmax(x, axis=-1):
    return torch.softmax(x, dim=axis)
def _gelu(x):
    return torch.nn.functional.gelu(x, approximate="tanh")
def _silu(x):
    return torch.nn.functional.silu(x)
def _sigmoid(x):
    return torch.sigmoid(x)
def _layernorm(x, axis=-1, eps=1e-5):
    m = x.mean(dim=axis, keepdim=True)
    v = x.var(dim=axis, keepdim=True, unbiased=False)
    return (x - m) / torch.sqrt(v + eps)
def _scatter_add(tgt, idx, src, axis=0):
    return tgt.clone().index_add_(axis, idx, src)
def _segment_sum(src, seg, n):
    out = torch.zeros((n,) + src.shape[1:], dtype=src.dtype)
    return out.index_add_(0, seg, src)
def _pad(x, pw):
    flat = tuple(v for pair in reversed(pw) for v in pair)
    return torch.nn.functional.pad(x, flat)

kernel_name = 'concat_heads__ssm_conv_h3__ssm_conv_s4__prompt'

def setup_inputs():

    _w = (lambda a: (lambda t: t.to(torch.bfloat16) if t.is_floating_point() else t)(torch.as_tensor(a)))
    def _A_setup():
        B, S, D = 1, 2048, 640
        ks = 4
        return {
            "q": _w((lambda *s: torch.randn(*s, dtype=torch.bfloat16) * 0.05)(B, S, D)),
            "k": _w((lambda *s: torch.randn(*s, dtype=torch.bfloat16) * 0.2)(B, S, D)),
            "v": _w((lambda *s: torch.randn(*s, dtype=torch.bfloat16) * 0.2)(B, S, D)),
            "shift_k": _w((lambda *s: torch.randn(*s, dtype=torch.bfloat16) * 0.2)(D, ks) * 0.5),
            "ssm_k": _w((lambda *s: torch.randn(*s, dtype=torch.bfloat16) * 0.8)(D, S) * (1.0 / S ** 0.5)),
            "ssm_D": _w((lambda *s: torch.randn(*s, dtype=torch.bfloat16) * 0.2)(D)),
        }
    _A_in = _A_setup()

    def _B_setup():
        B, L, D, N = 1, 2048, 640, 128
        A_re = -(lambda _x: torch.exp(torch.as_tensor(_x)))((lambda *s: torch.randn(*s, dtype=torch.bfloat16) * 3.2)(D, N))
        A_im = torch.pi * (lambda *s: torch.randn(*s, dtype=torch.bfloat16) * 0.05)(D, N)
        B_re = (lambda *s: torch.randn(*s, dtype=torch.bfloat16) * 0.1)(D, N)
        B_im = (lambda *s: torch.randn(*s, dtype=torch.bfloat16) * 0.1)(D, N)
        C_re = (lambda *s: torch.randn(*s, dtype=torch.bfloat16) * 0.1)(D, N)
        C_im = (lambda *s: torch.randn(*s, dtype=torch.bfloat16) * 0.1)(D, N)
        log_dt = (lambda *s: torch.randn(*s, dtype=torch.bfloat16) * 0.05)(D) * 0.5 - 4.0
        Dskip = (lambda *s: torch.randn(*s, dtype=torch.bfloat16) * 0.05)(D)
        return {
            "u": _w((lambda *s: torch.randn(*s, dtype=torch.bfloat16) * 0.05)(B, L, D)),
            "A_re": _w(A_re),
            "A_im": _w(A_im),
            "B_re": _w(B_re),
            "B_im": _w(B_im),
            "C_re": _w(C_re),
            "C_im": _w(C_im),
            "log_dt": _w(log_dt),
            "Dskip": _w(Dskip),
        }
    _B_in = _B_setup()

    return {
        "A_q": _A_in["q"],
        "A_k": _A_in["k"],
        "A_v": _A_in["v"],
        "A_shift_k": _A_in["shift_k"],
        "A_ssm_k": _A_in["ssm_k"],
        "A_ssm_D": _A_in["ssm_D"],
        "B_u": _B_in["u"],
        "B_A_re": _B_in["A_re"],
        "B_A_im": _B_in["A_im"],
        "B_B_re": _B_in["B_re"],
        "B_B_im": _B_in["B_im"],
        "B_C_re": _B_in["C_re"],
        "B_C_im": _B_in["C_im"],
        "B_log_dt": _B_in["log_dt"],
        "B_Dskip": _B_in["Dskip"],
    }

def reference(A_q, A_k, A_v, A_shift_k, A_ssm_k, A_ssm_D, B_u, B_A_re, B_A_im, B_B_re, B_B_im, B_C_re, B_C_im, B_log_dt, B_Dskip):
    def _A_ref(q, k, v, shift_k, ssm_k, ssm_D):
        B, S, D = q.shape
        ks = shift_k.shape[-1]
        k_pad = _pad(k, ((0, 0), (ks - 1, 0), (0, 0)))
        k_shift = torch.zeros_like(k)
        for t in range(ks):
            k_shift = k_shift + k_pad[:, t:t + S, :] * shift_k[:, ks - 1 - t][None, None, :]
        kv = k_shift * v
        L = 2 * S
        kv_pad = _pad(kv, ((0, 0), (0, S), (0, 0)))
        kf_pad = _pad(ssm_k, ((0, 0), (0, S)))
        kv_f = (lambda x, axes=(-2,-1): torch.fft.rfft2(x.float(), dim=axes))(kv_pad[:, None, :, :], axes=(1, 2))
        kf_f = (lambda x, axes=(-2,-1): torch.fft.rfft2(x.float(), dim=axes))(kf_pad[:, None, :], axes=(1, 2))
        kf_f = (lambda x, *d: x.permute(*d))(kf_f, 1, 2, 0)
        y_f = kv_f * kf_f[None, :, :, :]
        y = (lambda x, s, axes=(-2,-1): torch.fft.irfft2(x, s=s, dim=axes).to(torch.bfloat16))(y_f, (1, L), axes=(1, 2))[:, 0, :S, :]
        y = y + kv * ssm_D[None, None, :]
        return q * y

    def _B_ref(u, A_re, A_im, B_re, B_im, C_re, C_im, log_dt, Dskip):
        Bsz, L, D = u.shape
        dt = torch.exp(log_dt)[:, None]
        A = A_re + 1j * A_im
        Bc = B_re + 1j * B_im
        Cc = C_re + 1j * C_im
        logdA = dt * A
        dB = dt * Bc
        l = (lambda x, dt: x.to(dt))(torch.arange(L), A_re.dtype)
        P = torch.exp(logdA[:, :, None] * l[None, None, :])
        K = torch.einsum("dn,dnl,dn->dl", Cc, P, dB).real
        L2 = 2 * L
        Kp = _pad(K, ((0, 0), (0, L)))
        up = (lambda x, *d: x.permute(*d))(u, 0, 2, 1)
        up = _pad(up, ((0, 0), (0, 0), (0, L)))
        Kf = (lambda x, axes=(-2,-1): torch.fft.rfft2(x.float(), dim=axes))(Kp[:, None, :], axes=(-2, -1))
        Uf = (lambda x, axes=(-2,-1): torch.fft.rfft2(x.float(), dim=axes))(up[:, :, None, :], axes=(-2, -1))
        Yf = Uf * Kf[None, :, :, :]
        y = (lambda x, s, axes=(-2,-1): torch.fft.irfft2(x, s=s, dim=axes).to(torch.bfloat16))(Yf, (1, L2), axes=(-2, -1))[:, :, 0, :L]
        y = (lambda x, *d: x.permute(*d))(y, 0, 2, 1) + u * Dskip[None, None, :]
        return y

    a = _A_ref(A_q, A_k, A_v, A_shift_k, A_ssm_k, A_ssm_D)
    b = _B_ref(B_u, B_A_re, B_A_im, B_B_re, B_B_im, B_C_re, B_C_im, B_log_dt, B_Dskip)
    return torch.concatenate([a, b], axis=-1)
```

```cpp
#include <hip/hip_runtime.h>
#include <hip/hip_bf16.h>

typedef unsigned int u32;
typedef unsigned short u16;
typedef __attribute__((ext_vector_type(2))) u32 u32x2;
typedef __attribute__((ext_vector_type(4))) u32 u32x4;
typedef __attribute__((ext_vector_type(4))) short s16x4;
typedef __attribute__((ext_vector_type(8))) short s16x8;
typedef __attribute__((ext_vector_type(8))) __bf16 bf16x8;
typedef __attribute__((ext_vector_type(8))) _Float16 f16x8;
typedef __attribute__((ext_vector_type(2))) __bf16 bf16x2;
typedef __attribute__((ext_vector_type(2))) _Float16 f16x2;
typedef __attribute__((ext_vector_type(2))) float f32x2;
typedef __attribute__((ext_vector_type(4))) float f32x4;
#define LDS_AS __attribute__((address_space(3)))

constexpr int S = 2048;
constexpr int D = 640;
constexpr int NM = 128;

__device__ __forceinline__ float bf_lo(u32 v) { return __uint_as_float(v << 16); }
__device__ __forceinline__ float bf_hi(u32 v) { return __uint_as_float(v & 0xffff0000u); }
__device__ __forceinline__ float bf2f(u16 v) { return __uint_as_float(((u32)v) << 16); }
__device__ __forceinline__ u32 pk_bf16(float a, float b) {
  f32x2 v = {a, b};
  return __builtin_bit_cast(u32, __builtin_convertvector(v, bf16x2));
}
__device__ __forceinline__ u32 pk_f16(float a, float b) {
  f32x2 v = {a, b};
  return __builtin_bit_cast(u32, __builtin_convertvector(v, f16x2));
}
__device__ __forceinline__ float rbf(float a) { return bf_lo(pk_bf16(a, a)); }

constexpr size_t WS_XT = 0;
constexpr size_t WS_KT = WS_XT + (size_t)1280 * S * 2;
constexpr size_t WS_KS = WS_KT + (size_t)640 * S * 2;
constexpr size_t WS_YT = WS_KS + 4096;

__device__ __forceinline__ void cmul(float& or_, float& oi, float ar, float ai, float br, float bi) {
  float r = ar * br - ai * bi;
  float i = ar * bi + ai * br;
  or_ = r; oi = i;
}
__device__ __forceinline__ void cpow(float& zr, float& zi, float a, float b, float m) {
  float e = __builtin_amdgcn_exp2f((a * m) * 1.44269504088896341f);
  float th = b * m;
  const float I2PH = 0.15915494309189535f;
  const float I2PL = 6.4206383e-09f;
  float k = __builtin_rintf(th * I2PH);
  float f = __builtin_fmaf(th, I2PH, -k);
  f = __builtin_fmaf(th, I2PL, f);
  zr = e * __builtin_amdgcn_cosf(f);
  zi = e * __builtin_amdgcn_sinf(f);
}

__device__ __forceinline__ float wave_max(float v) {
#pragma unroll
  for (int o = 32; o >= 1; o >>= 1) v = fmaxf(v, __shfl_xor(v, o));
  return v;
}

__device__ __forceinline__ void pow2_scale(float m, int T, float& s, float& inv) {
  int e = (int)((__float_as_uint(m) >> 23) & 0xff);
  if (e == 0) e = 1;
  if (e == 255) e = 254;
  int se = 127 + T - (e - 127);
  se = se < 1 ? 1 : (se > 253 ? 253 : se);
  s = __uint_as_float((u32)se << 23);
  inv = __uint_as_float((u32)(254 - se) << 23);
}

__global__ __launch_bounds__(256) void k_prep(
    const u16* __restrict__ kk, const u16* __restrict__ vv, const u16* __restrict__ shk,
    const u16* __restrict__ uu,
    const u16* __restrict__ Are, const u16* __restrict__ Aim, const u16* __restrict__ Bre, const u16* __restrict__ Bim,
    const u16* __restrict__ Cre, const u16* __restrict__ Cim, const u16* __restrict__ logdt,
    char* __restrict__ ws) {
  __shared__ __attribute__((aligned(16))) char smem[4 * 33280];
  const int b = blockIdx.x, tid = threadIdx.x;
  if (b < 160) {
    const bool isA = b < 80;
    const int bb = isA ? b : b - 80;
    const int slab = bb % 10, tt = bb / 10;
    const int co = tid & 7, s = tid >> 3;
    const int ch0 = slab * 64 + co * 8;
    const int t0 = tt * 256 + s * 8;
    u32 o[8][4];
    if (isA) {
      u32x4 kr[11], vr[8];
#pragma unroll
      for (int r = 0; r < 11; ++r) {
        int t = t0 - 3 + r;
        u32x4 z = {0, 0, 0, 0};
        kr[r] = (t >= 0) ? *(const u32x4*)(kk + (size_t)t * D + ch0) : z;
      }
#pragma unroll
      for (int r = 0; r < 8; ++r) vr[r] = *(const u32x4*)(vv + (size_t)(t0 + r) * D + ch0);
      u32x4 w0 = *(const u32x4*)(shk + ch0 * 4), w1 = *(const u32x4*)(shk + ch0 * 4 + 8);
      u32x4 w2 = *(const u32x4*)(shk + ch0 * 4 + 16), w3 = *(const u32x4*)(shk + ch0 * 4 + 24);
      u32 wv[16] = {w0.x, w0.y, w0.z, w0.w, w1.x, w1.y, w1.z, w1.w, w2.x, w2.y, w2.z, w2.w, w3.x, w3.y, w3.z, w3.w};
#pragma unroll
      for (int c = 0; c < 8; ++c) {
        float s0 = bf_lo(wv[2 * c]), s1 = bf_hi(wv[2 * c]), s2 = bf_lo(wv[2 * c + 1]), s3 = bf_hi(wv[2 * c + 1]);
        float kf[11], kvf[8];
#pragma unroll
        for (int r = 0; r < 11; ++r) { u32 w = kr[r][c >> 1]; kf[r] = (c & 1) ? bf_hi(w) : bf_lo(w); }
#pragma unroll
        for (int r = 0; r < 8; ++r) {
          float a = rbf(kf[r] * s3);
          a = rbf(a + rbf(kf[r + 1] * s2));
          a = rbf(a + rbf(kf[r + 2] * s1));
          a = rbf(a + rbf(kf[r + 3] * s0));
          u32 w = vr[r][c >> 1];
          kvf[r] = a * ((c & 1) ? bf_hi(w) : bf_lo(w));
        }
#pragma unroll
        for (int p = 0; p < 4; ++p) o[c][p] = pk_bf16(kvf[p], kvf[p + 4]);
      }
    } else {
      u32x4 ur[8];
#pragma unroll
      for (int r = 0; r < 8; ++r) ur[r] = *(const u32x4*)(uu + (size_t)(t0 + r) * D + ch0);
#pragma unroll
      for (int c = 0; c < 8; ++c)
#pragma unroll
        for (int p = 0; p < 4; ++p) {
          u32 a = ur[p][c >> 1], bq = ur[p + 4][c >> 1];
          o[c][p] = (c & 1) ? ((a >> 16) | (bq & 0xffff0000u)) : ((a & 0xffffu) | (bq << 16));
        }
    }
    u32* sm = (u32*)smem;
#pragma unroll
    for (int c = 0; c < 8; ++c)
#pragma unroll
      for (int p = 0; p < 4; ++p) sm[((co * 8 + c) * 4 + p) * 32 + co * 4 + s] = o[c][p];
    __syncthreads();
    u16* xt = (u16*)(ws + WS_XT);
#pragma unroll
    for (int it = 0; it < 8; ++it) {
      int idx = it * 256 + tid;
      int row = idx >> 3, chunk = idx & 7;
      int ch = row >> 2, p = row & 3;
      u32x4 val = *(const u32x4*)(sm + row * 32 + (ch >> 3) * 4 + chunk * 4);
      int gch = (isA ? 0 : 640) + slab * 64 + ch;
      *(u32x4*)(xt + ((size_t)gch * 4 + p) * 512 + tt * 64 + chunk * 8) = val;
    }
    return;
  }
  const int wave = tid >> 6, lane = tid & 63;
  const int d = (b - 160) * 4 + wave;
  constexpr int RS = 132;
  u32* E = (u32*)(smem + wave * 33280);
  u32* F = E + 48 * RS;
  F = E + 42 * RS;
  const float dt = rbf(expf(bf2f(logdt[d])));
  float cr[2], ci[2], aa[2], bbv[2];
  float cm = 0.f;
#pragma unroll
  for (int m = 0; m < 2; ++m) {
    int n = lane + 64 * m;
    aa[m] = dt * bf2f(Are[d * NM + n]);
    bbv[m] = dt * bf2f(Aim[d * NM + n]);
    float br = dt * bf2f(Bre[d * NM + n]), bi = dt * bf2f(Bim[d * NM + n]);
    cmul(cr[m], ci[m], bf2f(Cre[d * NM + n]), bf2f(Cim[d * NM + n]), br, bi);
    cm = fmaxf(cm, fmaxf(fabsf(cr[m]), fabsf(ci[m])));
  }
  cm = wave_max(cm);
  float cs, cinv;
  pow2_scale(cm, 8, cs, cinv);
#pragma unroll
  for (int m = 0; m < 2; ++m) {
    int n = lane + 64 * m;
    float a = aa[m], bq = bbv[m];
    float z1r, z1i, z8r, z8i, v1r, v1i, v2r, v2i, v4r, v4i, v8r, v8i;
    cpow(z1r, z1i, a, bq, 1.f);
    cpow(z8r, z8i, a, bq, 8.f);
    cpow(v1r, v1i, a, bq, 128.f);
    cpow(v2r, v2i, a, bq, 256.f);
    cpow(v4r, v4i, a, bq, 512.f);
    cpow(v8r, v8i, a, bq, 1024.f);
    float fr = 1.f, fi = 0.f;
#pragma unroll
    for (int j = 0; j < 16; ++j) {
      F[j * RS + n] = pk_f16(fr, -fi);
      cmul(fr, fi, fr, fi, z8r, z8i);
    }
    float gr[8], gi[8];
    gr[0] = cr[m] * cs; gi[0] = ci[m] * cs;
#pragma unroll
    for (int p = 1; p < 8; ++p) cmul(gr[p], gi[p], gr[p - 1], gi[p - 1], z1r, z1i);
#pragma unroll
    for (int p = 0; p < 8; ++p) {
      E[p * RS + n] = pk_f16(gr[p], gi[p]);
      float tr, ti; cmul(tr, ti, gr[p], gi[p], v1r, v1i);
      E[(8 + p) * RS + n] = pk_f16(tr, ti);
    }
#pragma unroll
    for (int p = 0; p < 4; ++p) {
      float tr, ti; cmul(tr, ti, gr[2 * p], gi[2 * p], v2r, v2i);
      E[(16 + p) * RS + n] = pk_f16(tr, ti);
      cmul(tr, ti, tr, ti, v1r, v1i);
      E[(20 + p) * RS + n] = pk_f16(tr, ti);
    }
#pragma unroll
    for (int p = 0; p < 2; ++p) {
      float tr, ti; cmul(tr, ti, gr[4 * p], gi[4 * p], v4r, v4i);
#pragma unroll
      for (int a2 = 0; a2 < 4; ++a2) {
        E[(24 + 2 * a2 + p) * RS + n] = pk_f16(tr, ti);
        if (a2 < 3) cmul(tr, ti, tr, ti, v1r, v1i);
      }
    }
    {
      float tr, ti; cmul(tr, ti, gr[0], gi[0], v8r, v8i);
#pragma unroll
      for (int a2 = 0; a2 < 9; ++a2) {
        E[(32 + a2) * RS + n] = pk_f16(tr, ti);
        if (a2 < 8) cmul(tr, ti, tr, ti, v1r, v1i);
      }
    }
    E[41 * RS + n] = 0;
  }
  __builtin_amdgcn_fence(__ATOMIC_RELEASE, "wavefront");
  __builtin_amdgcn_wave_barrier();
  f32x4 acc[3] = {{0, 0, 0, 0}, {0, 0, 0, 0}, {0, 0, 0, 0}};
  const int r16 = lane & 15, kg = lane >> 4;
#pragma unroll
  for (int ks = 0; ks < 8; ++ks) {
    f16x8 fb = *(const f16x8*)(F + r16 * RS + ks * 16 + kg * 4);
#pragma unroll
    for (int t = 0; t < 3; ++t) {
      int row = t * 16 + r16; row = row > 41 ? 41 : row;
      f16x8 ea = *(const f16x8*)(E + row * RS + ks * 16 + kg * 4);
      acc[t] = __builtin_amdgcn_mfma_f32_16x16x32_f16(ea, fb, acc[t], 0, 0, 0);
    }
  }
  float km = 0.f;
#pragma unroll
  for (int t = 0; t < 3; ++t)
#pragma unroll
    for (int i = 0; i < 4; ++i) {
      int row = 16 * t + 4 * kg + i;
      bool valid = (row < 40) || (row == 40 && r16 == 0);
      km = fmaxf(km, valid ? fabsf(acc[t][i]) : 0.f);
    }
  km = wave_max(km);
  float ks2, kinv;
  pow2_scale(km, 12, ks2, kinv);
  __builtin_amdgcn_fence(__ATOMIC_RELEASE, "wavefront");
  __builtin_amdgcn_wave_barrier();
  u16* Q = (u16*)E;
#pragma unroll
  for (int t = 0; t < 3; ++t)
#pragma unroll
    for (int i = 0; i < 4; ++i) {
      int row = 16 * t + 4 * kg + i;
      int hi;
      if (row < 16) hi = (row & 7) + 128 * (row >> 3);
      else if (row < 24) hi = 256 + 2 * (row & 3) + 128 * ((row >> 2) & 1);
      else if (row < 32) hi = 512 + 4 * (row & 1) + 128 * ((row >> 1) & 3);
      else hi = 1024 + 128 * (row - 32);
      int l = hi + 8 * r16;
      bool valid = (row < 40) || (row == 40 && r16 == 0);
      if (valid) Q[l] = (u16)(pk_f16(acc[t][i] * ks2, 0.f) & 0xffffu);
    }
  __builtin_amdgcn_fence(__ATOMIC_RELEASE, "wavefront");
  __builtin_amdgcn_wave_barrier();
  u16* kt = (u16*)(ws + WS_KT) + (size_t)d * S;
#pragma unroll
  for (int it = 0; it < 4; ++it) {
    int l0 = (it * 64 + lane) * 8;
    u32 w[4];
#pragma unroll
    for (int e = 0; e < 4; ++e) {
      int la = l0 + 2 * e, lb = la + 1;
      int qa = (int)rbf((float)la), qb = (int)rbf((float)lb);
      w[e] = (u32)Q[qa] | ((u32)Q[qb] << 16);
    }
    u32x4 val = {w[0], w[1], w[2], w[3]};
    *(u32x4*)(kt + l0) = val;
  }
  if (lane == 0) ((float*)(ws + WS_KS))[d] = kinv * cinv;
}

constexpr int HS_BYTES = 4128;
constexpr int XP_BYTES = 1152;
constexpr int CW_BYTES = HS_BYTES + 4 * XP_BYTES;

template <bool F16>
__device__ __forceinline__ f32x4 mfma16(s16x8 a, s16x8 b, f32x4 c) {
  if (F16) return __builtin_amdgcn_mfma_f32_16x16x32_f16(__builtin_bit_cast(f16x8, a), __builtin_bit_cast(f16x8, b), c, 0, 0, 0);
  else return __builtin_amdgcn_mfma_f32_16x16x32_bf16(__builtin_bit_cast(bf16x8, a), __builtin_bit_cast(bf16x8, b), c, 0, 0, 0);
}

template <bool F16>
__device__ __forceinline__ void conv_core(char* hs, char* xs, int lane, f32x4 (&acc)[4][8]) {
  const int kg = lane >> 4, q = (lane & 15) >> 2, p = lane & 3, n = lane & 15;
  s16x8 A[16];
  {
    const int base = 256 - 64 * kg - 8 * q + 8 * p;
#pragma unroll
    for (int e = 0; e < 16; ++e) {
      s16x4 lo = __builtin_amdgcn_ds_read_tr16_b64_v4i16((LDS_AS s16x4*)(hs + base + 256 * e));
      s16x4 hi = __builtin_amdgcn_ds_read_tr16_b64_v4i16((LDS_AS s16x4*)(hs + base + 256 * e - 32));
      A[e] = __builtin_shufflevector(lo, hi, 0, 1, 2, 3, 4, 5, 6, 7);
    }
  }
#pragma unroll
  for (int c = 0; c < 4; ++c)
#pragma unroll
    for (int r = 0; r < 8; ++r) acc[c][r] = (f32x4){0.f, 0.f, 0.f, 0.f};
  const int bbase = 8 + 16 * kg + 8 * n;
#pragma unroll
  for (int jj = 0; jj < 16; ++jj) {
#pragma unroll
    for (int c = 0; c < 4; ++c) {
      const char* pb = xs + c * XP_BYTES + bbase + 64 * jj;
      s16x4 lo = *(const s16x4*)(pb);
      s16x4 hi = *(const s16x4*)(pb + 8);
      s16x8 B = __builtin_shufflevector(lo, hi, 0, 1, 2, 3, 4, 5, 6, 7);
#pragma unroll
      for (int r = 0; r < 8; ++r) {
        const int e = 2 * r - (jj - 1);
        if (e >= 0 && e < 16) acc[c][r] = mfma16<F16>(A[e], B, acc[c][r]);
      }
    }
  }
}

__global__ __launch_bounds__(256, 2) void k_conv(const u16* __restrict__ ssmk, const u16* __restrict__ ssmD,
                                                 const u16* __restrict__ Dskip, char* __restrict__ ws) {
  __shared__ __attribute__((aligned(16))) char smem[4 * CW_BYTES];
  const int wave = threadIdx.x >> 6, lane = threadIdx.x & 63;
  const int ch = blockIdx.x * 4 + wave;
  const bool isA = ch < 640;
  char* hs = smem + wave * CW_BYTES;
  char* xs = hs + HS_BYTES;
  const u16* hsrc = isA ? (ssmk + (size_t)ch * S) : ((const u16*)(ws + WS_KT) + (size_t)(ch - 640) * S);
  const u16* xsrc = (const u16*)(ws + WS_XT) + (size_t)ch * S;
  u32x4 hv[4], xv[4];
#pragma unroll
  for (int m = 0; m < 4; ++m) hv[m] = *(const u32x4*)(hsrc + (m * 64 + lane) * 8);
#pragma unroll
  for (int m = 0; m < 4; ++m) xv[m] = *(const u32x4*)(xsrc + (m * 64 + lane) * 8);
  const float dsk = bf2f(isA ? ssmD[ch] : Dskip[ch - 640]);
  float oscale = 1.f;
  if (lane < 2) *(u32x4*)(hs + lane * 16) = (u32x4){0, 0, 0, 0};
  if (lane < 32) *(u32x4*)(xs + (lane >> 3) * XP_BYTES + (lane & 7) * 16) = (u32x4){0, 0, 0, 0};
#pragma unroll
  for (int m = 0; m < 4; ++m) *(u32x4*)(hs + 32 + (m * 64 + lane) * 16) = hv[m];
  if (isA) {
#pragma unroll
    for (int m = 0; m < 4; ++m) *(u32x4*)(xs + m * XP_BYTES + 128 + lane * 16) = xv[m];
  } else {
    u32 mx = 0;
#pragma unroll
    for (int m = 0; m < 4; ++m)
#pragma unroll
      for (int e = 0; e < 4; ++e) {
        u32 w = xv[m][e];
        u32 a = (w << 16) & 0x7fff0000u, b2 = w & 0x7fff0000u;
        mx = max(mx, max(a, b2));
      }
    float um = wave_max(__uint_as_float(mx));
    float us, uinv;
    pow2_scale(um, 12, us, uinv);
    oscale = uinv * ((const float*)(ws + WS_KS))[ch - 640];
#pragma unroll
    for (int m = 0; m < 4; ++m) {
      u32x4 o;
#pragma unroll
      for (int e = 0; e < 4; ++e) o[e] = pk_f16(bf_lo(xv[m][e]) * us, bf_hi(xv[m][e]) * us);
      *(u32x4*)(xs + m * XP_BYTES + 128 + lane * 16) = o;
    }
  }
  __builtin_amdgcn_fence(__ATOMIC_RELEASE, "wavefront");
  __builtin_amdgcn_wave_barrier();
  f32x4 acc[4][8];
  if (isA) conv_core<false>(hs, xs, lane, acc);
  else conv_core<true>(hs, xs, lane, acc);
  __builtin_amdgcn_fence(__ATOMIC_RELEASE, "wavefront");
  __builtin_amdgcn_wave_barrier();
  const int n = lane & 15, rg = lane >> 4;
  const int src = (lane >= 16) ? (lane - 16) : ((lane + 47) & 63);
  float pc0 = 0.f, pc1 = 0.f, pc2 = 0.f;
#pragma unroll
  for (int r = 0; r < 8; ++r) {
    float c0 = acc[1][r][3] + acc[2][r][2] + acc[3][r][1];
    float c1 = acc[2][r][3] + acc[3][r][2];
    float c2 = acc[3][r][3];
    float i0 = __shfl(c0, src), i1 = __shfl(c1, src), i2 = __shfl(c2, src);
    if (lane == 0) { i0 = pc0; i1 = pc1; i2 = pc2; }
    pc0 = __shfl(c0, 63); pc1 = __shfl(c1, 63); pc2 = __shfl(c2, 63);
    float y0 = acc[0][r][0] + i0;
    float y1 = acc[0][r][1] + acc[1][r][0] + i1;
    float y2 = acc[0][r][2] + acc[1][r][1] + acc[2][r][0] + i2;
    float y3 = acc[0][r][3] + acc[1][r][2] + acc[2][r][1] + acc[3][r][0];
    u32x2 o = {pk_bf16(y0 * oscale, y1 * oscale), pk_bf16(y2 * oscale, y3 * oscale)};
    *(u32x2*)(hs + 512 * r + 32 * n + 8 * rg) = o;
  }
  __builtin_amdgcn_fence(__ATOMIC_RELEASE, "wavefront");
  __builtin_amdgcn_wave_barrier();
  u16* yt = (u16*)(ws + WS_YT) + (size_t)ch * S + lane * 32;
  asm volatile("" ::: "memory");
#pragma unroll
  for (int m = 0; m < 4; ++m) xv[m] = *(const volatile u32x4*)(xsrc + (m * 64 + lane) * 8);
#pragma unroll
  for (int g = 0; g < 4; ++g) {
    u32x4 yv = *(const u32x4*)(hs + lane * 64 + g * 16);
    u32x4 o;
#pragma unroll
    for (int e = 0; e < 4; ++e) {
      int ii = e >> 1;
      int c0 = (2 * e) & 3, c1 = c0 + 1;
      u32 w0 = xv[c0][g], w1 = xv[c1][g];
      float x0 = ii ? bf_hi(w0) : bf_lo(w0), x1 = ii ? bf_hi(w1) : bf_lo(w1);
      u32 xd = pk_bf16(x0 * dsk, x1 * dsk);
      o[e] = pk_bf16(bf_lo(yv[e]) + bf_lo(xd), bf_hi(yv[e]) + bf_hi(xd));
    }
    *(u32x4*)(yt + g * 8) = o;
  }
}

__global__ __launch_bounds__(256) void k_out(const u16* __restrict__ qq, const char* __restrict__ ws, u16* __restrict__ out) {
  __shared__ __attribute__((aligned(16))) u16 sm[64 * 136];
  const int b = blockIdx.x, tid = threadIdx.x;
  const int slab = b % 20, tt = b / 20;
  const u16* yt = (const u16*)(ws + WS_YT);
  const int co = tid & 7, tr = tid >> 3;
  u32x4 qv[4];
  const bool isA = slab < 10;
  if (isA) {
#pragma unroll
    for (int i = 0; i < 4; ++i) qv[i] = *(const u32x4*)(qq + (size_t)(tt * 128 + tr + 32 * i) * D + slab * 64 + co * 8);
  }
#pragma unroll
  for (int it = 0; it < 4; ++it) {
    int idx = it * 256 + tid;
    int ch = idx >> 4, chunk = idx & 15;
    u32x4 v = *(const u32x4*)(yt + (size_t)(slab * 64 + ch) * S + tt * 128 + chunk * 8);
    *(u32x4*)(sm + ch * 136 + chunk * 8) = v;
  }
  __syncthreads();
#pragma unroll
  for (int i = 0; i < 4; ++i) {
    int t = tr + 32 * i;
    u32x4 o;
#pragma unroll
    for (int e = 0; e < 4; ++e) {
      u32 a = sm[(co * 8 + 2 * e) * 136 + t], c = sm[(co * 8 + 2 * e + 1) * 136 + t];
      if (isA) o[e] = pk_bf16(bf_lo(a) * bf_lo(qv[i][e]), bf_lo(c) * bf_hi(qv[i][e]));
      else o[e] = a | (c << 16);
    }
    *(u32x4*)(out + (size_t)(tt * 128 + t) * 1280 + slab * 64 + co * 8) = o;
  }
}

extern "C" void kernel_launch(void* const* d_in, const int* in_sizes, int n_in,
                              void* d_out, int out_size, void* d_ws, size_t ws_size,
                              hipStream_t stream) {
  const u16* q = (const u16*)d_in[0];
  const u16* k = (const u16*)d_in[1];
  const u16* v = (const u16*)d_in[2];
  const u16* shk = (const u16*)d_in[3];
  const u16* ssmk = (const u16*)d_in[4];
  const u16* ssmD = (const u16*)d_in[5];
  const u16* u = (const u16*)d_in[6];
  char* ws = (char*)d_ws;
  k_prep<<<320, 256, 0, stream>>>(k, v, shk, u, (const u16*)d_in[7], (const u16*)d_in[8], (const u16*)d_in[9],
                                  (const u16*)d_in[10], (const u16*)d_in[11], (const u16*)d_in[12],
                                  (const u16*)d_in[13], ws);
  k_conv<<<320, 256, 0, stream>>>(ssmk, ssmD, (const u16*)d_in[14], ws);
  k_out<<<320, 256, 0, stream>>>(q, ws, (u16*)d_out);
}
```

```cpp
#include <hip/hip_runtime.h>
#include <hip/hip_bf16.h>

typedef unsigned int u32;
typedef unsigned short u16;
typedef __attribute__((ext_vector_type(2))) u32 u32x2;
typedef __attribute__((ext_vector_type(4))) u32 u32x4;
typedef __attribute__((ext_vector_type(4))) short s16x4;
typedef __attribute__((ext_vector_type(8))) short s16x8;
typedef __attribute__((ext_vector_type(8))) __bf16 bf16x8;
typedef __attribute__((ext_vector_type(8))) _Float16 f16x8;
typedef __attribute__((ext_vector_type(2))) __bf16 bf16x2;
typedef __attribute__((ext_vector_type(2))) _Float16 f16x2;
typedef __attribute__((ext_vector_type(2))) unsigned short u16x2;
typedef __attribute__((ext_vector_type(2))) float f32x2;
typedef __attribute__((ext_vector_type(4))) float f32x4;
#define LDS_AS __attribute__((address_space(3)))
typedef LDS_AS char* lptr;

#define STAMPW(w, idx)

#define UPOL " nt"
#define KVPOL " nt"
#define D1DELAY 10
#define D0DELAY 1
#define UDELAY 20
#define PREFETCH 1
#define SLEEPN 1
#define EXACT_ROUND 1

constexpr int S = 2048;
constexpr int D = 640;
constexpr int NM = 128;

__device__ __forceinline__ float bf_lo(u32 v) { return __uint_as_float(v << 16); }
__device__ __forceinline__ float bf_hi(u32 v) { return __uint_as_float(v & 0xffff0000u); }
__device__ __forceinline__ u32 pk_bf16(float a, float b) {
  f32x2 v = {a, b};
  return __builtin_bit_cast(u32, __builtin_convertvector(v, bf16x2));
}
__device__ __forceinline__ u32 pk_f16(float a, float b) {
  f32x2 v = {a, b};
  return __builtin_bit_cast(u32, __builtin_convertvector(v, f16x2));
}
__device__ __forceinline__ u32 pk_f16v(f32x2 v) { return __builtin_bit_cast(u32, __builtin_convertvector(v, f16x2)); }
__device__ __forceinline__ float rbf(float a) { return bf_lo(pk_bf16(a, a)); }
__device__ __forceinline__ void rbf2(float& a, float& b) { u32 p = pk_bf16(a, b); a = bf_lo(p); b = bf_hi(p); }

#define WAITV(n) asm volatile("s_waitcnt vmcnt(" #n ")" ::: "memory")
#define BAR() do { asm volatile("s_waitcnt lgkmcnt(0)" ::: "memory"); __builtin_amdgcn_s_barrier(); asm volatile("" ::: "memory"); } while (0)
#define CFENCE() asm volatile("" ::: "memory")

__device__ __forceinline__ void dma16(const void* g, u32 lds) {
  u32 keep;
  lds = __builtin_amdgcn_readfirstlane(lds);
  asm volatile("s_mov_b32 %0, m0\n\ts_mov_b32 m0, %2\n\ts_nop 0\n\tglobal_load_lds_dwordx4 %1, off" KVPOL "\n\ts_mov_b32 m0, %0"
               : "=&s"(keep) : "v"(g), "s"(lds) : "memory");
}
__device__ __forceinline__ void dma4(const void* g, u32 lds) {
  u32 keep;
  lds = __builtin_amdgcn_readfirstlane(lds);
  asm volatile("s_mov_b32 %0, m0\n\ts_mov_b32 m0, %2\n\ts_nop 0\n\tglobal_load_lds_dword %1, off nt\n\ts_mov_b32 m0, %0"
               : "=&s"(keep) : "v"(g), "s"(lds) : "memory");
}
__device__ __forceinline__ u32x2 tr_read(lptr p) {
  return __builtin_bit_cast(u32x2, __builtin_amdgcn_ds_read_tr16_b64_v4i16((LDS_AS s16x4*)p));
}
__device__ __forceinline__ void wait_cnt(lptr p, u32 target) {
  CFENCE();
  while ((u32)__builtin_amdgcn_readfirstlane(*(volatile LDS_AS u32*)p) < target) __builtin_amdgcn_s_sleep(SLEEPN);
  CFENCE();
}
__device__ __forceinline__ void signal(lptr p, int lane) {
  CFENCE();
  if (lane == 0) __hip_atomic_fetch_add((LDS_AS u32*)p, 1u, __ATOMIC_RELAXED, __HIP_MEMORY_SCOPE_WORKGROUP);
  CFENCE();
}

__device__ __forceinline__ void signal_nb(lptr p) {
  asm volatile("s_mov_b64 exec, 1\n\tds_add_u32 %0, %1\n\ts_mov_b64 exec, -1" :: "v"((u32)(size_t)p), "v"(1u) : "memory");
}

constexpr int XP = 1152;
constexpr int XS_CH = 4640;
constexpr int XS2 = 8 * XS_CH + 128 + 8;
constexpr int XS_ALL = 2 * 8 * XS_CH + 256;
constexpr int HS_CH = 4128;

template <bool F16>
__device__ __forceinline__ f32x4 mfma16(s16x8 a, s16x8 b, f32x4 c) {
  if (F16) return __builtin_amdgcn_mfma_f32_16x16x32_f16(__builtin_bit_cast(f16x8, a), __builtin_bit_cast(f16x8, b), c, 0, 0, 0);
  else return __builtin_amdgcn_mfma_f32_16x16x32_bf16(__builtin_bit_cast(bf16x8, a), __builtin_bit_cast(bf16x8, b), c, 0, 0, 0);
}

__device__ __forceinline__ float fadd(float a, float b) { float r; asm("v_add_f32 %0, %1, %2" : "=v"(r) : "v"(a), "v"(b)); return r; }
__device__ __forceinline__ float fmul(float a, float b) { float r; asm("v_mul_f32 %0, %1, %2" : "=v"(r) : "v"(a), "v"(b)); return r; }
__device__ __forceinline__ void sw16(float& a, float& b) {
  auto r = __builtin_amdgcn_permlane16_swap(__float_as_uint(a), __float_as_uint(b), false, false);
  a = __uint_as_float(r[0]); b = __uint_as_float(r[1]);
}
__device__ __forceinline__ void sw32(float& a, float& b) {
  auto r = __builtin_amdgcn_permlane32_swap(__float_as_uint(a), __float_as_uint(b), false, false);
  a = __uint_as_float(r[0]); b = __uint_as_float(r[1]);
}
__device__ __forceinline__ void tr4(float& a, float& b, float& c, float& d) { sw32(a, c); sw32(b, d); sw16(a, b); sw16(c, d); }

struct Stream {
  s16x8 A[16];
  f32x4 acc[4][2];
  s16x8 B[3];
  float c0, c1, c2, c3, l0, l1, l2, l3, sv;
  u32 cnt;
};
constexpr int pair_of(int F) { return F < 16 ? 0 : (F < 48 ? 1 : (F < 96 ? 2 : 3)); }
constexpr int pair_base(int a) { return a == 0 ? 0 : (a == 1 ? 16 : (a == 2 ? 48 : (a == 3 ? 96 : 160))); }
constexpr int frag_off(int F) { return ((F - pair_base(pair_of(F))) & 3) * XP + 64 * ((F - pair_base(pair_of(F))) >> 2); }
constexpr int first_use(int ch) { return pair_base(ch >> 1) + 8 * ch; }
constexpr int chunk_first_used_at(int F) {
  for (int ch = 1; ch < 8; ++ch) if (first_use(ch) == F) return ch;
  return -1;
}
template <int r> __device__ __forceinline__ void mrg1(Stream& st) {
  st.c0 = fadd(fadd(st.acc[1][r][3], st.acc[2][r][2]), st.acc[3][r][1]);
  st.c1 = fadd(st.acc[2][r][3], st.acc[3][r][2]);
  st.c2 = st.acc[3][r][3];
  st.l0 = st.acc[0][r][0];
  st.l1 = fadd(st.acc[0][r][1], st.acc[1][r][0]);
  st.l2 = fadd(fadd(st.acc[0][r][2], st.acc[1][r][1]), st.acc[2][r][0]);
  st.l3 = fadd(fadd(st.acc[0][r][3], st.acc[1][r][2]), fadd(st.acc[2][r][1], st.acc[3][r][0]));
}
__device__ __forceinline__ void mrg2(Stream& st) {
  st.c3 = 0.f;
  tr4(st.l0, st.l1, st.l2, st.l3);
  tr4(st.c0, st.c1, st.c2, st.c3);
}
template <int T, bool SC> __device__ __forceinline__ void mrg3(Stream& st, lptr yp, float oscale) {
  float prev = __uint_as_float(__builtin_amdgcn_update_dpp(__float_as_uint(st.sv), __float_as_uint(st.c3), 0x111, 0xf, 0xf, false));
  st.sv = __uint_as_float(__builtin_amdgcn_update_dpp(0u, __float_as_uint(st.c3), 0x121, 0xf, 0xf, false));
  float y0 = fadd(st.l0, prev), y1 = fadd(st.l1, st.c0), y2 = fadd(st.l2, st.c1), y3 = fadd(st.l3, st.c2);
  if (SC) { y0 = fmul(y0, oscale); y1 = fmul(y1, oscale); y2 = fmul(y2, oscale); y3 = fmul(y3, oscale); }
  u32x2 w = {pk_bf16(y0, y1), pk_bf16(y2, y3)};
  *(LDS_AS u32x2*)(yp + 1024 * (T >> 1) + 256 * (T & 1)) = w;
}
template <int E> __device__ __forceinline__ void load_afrag(Stream& st, lptr ab) {
  s16x4 lo = __builtin_amdgcn_ds_read_tr16_b64_v4i16((LDS_AS s16x4*)(ab + 256 * E));
  s16x4 hi = __builtin_amdgcn_ds_read_tr16_b64_v4i16((LDS_AS s16x4*)(ab + 256 * E - 32));
  st.A[E] = __builtin_shufflevector(lo, hi, 0, 1, 2, 3, 4, 5, 6, 7);
}
#define PIN_ACC(st) asm volatile("" : "+v"(st.acc[0][0]), "+v"(st.acc[1][0]), "+v"(st.acc[2][0]), "+v"(st.acc[3][0]), \
                                      "+v"(st.acc[0][1]), "+v"(st.acc[1][1]), "+v"(st.acc[2][1]), "+v"(st.acc[3][1]))
template <bool F16, bool STREAMED, int F>
__device__ __forceinline__ void conv_step(Stream& st, lptr xb, lptr ab, lptr yp, lptr cx, lptr cy, int lane, float oscale, char* ws) {
  constexpr int PD = 2;
  constexpr int a = pair_of(F), rel = F - pair_base(a), jj = rel >> 2, c = rel & 3;
  constexpr int nrel = pair_base(a + 1) - pair_base(a);
  if constexpr (STREAMED) {
    constexpr int chE = chunk_first_used_at(F + PD + 3), chN = chunk_first_used_at(F + PD);
    if constexpr (chE > 0) st.cnt = *(volatile LDS_AS u32*)(cx + 4 * (chE > 0 ? chE : 0));
    if constexpr (chN > 0) {
      if ((u32)__builtin_amdgcn_readfirstlane(st.cnt) < 4u) {
        if constexpr (F >= 4) PIN_ACC(st);
        wait_cnt(cx + 4 * (chN > 0 ? chN : 0), 4);
      }
      CFENCE();
    }
    if constexpr (a < 3 && rel == 4) { if (a == 0) WAITV(2); else if (a == 1) WAITV(1); else WAITV(0); }
  }
  if constexpr (STREAMED && rel == 3) { if (a == 2) __builtin_amdgcn_s_setprio(1); if (a == 3) __builtin_amdgcn_s_setprio(0); }
  if constexpr (F + PD < 160) st.B[(F + PD) % 3] = *(volatile LDS_AS s16x8*)(xb + frag_off(F + PD));
  if constexpr (a < 3 && rel >= 4 && rel < 8) load_afrag<(a < 3 ? 4 * (a + 1) + (rel & 3) : 0)>(st, ab);
  if constexpr (a > 0 && rel == 0) { asm volatile("s_nop 6" : "+v"(st.acc[3][1])); mrg1<1>(st); }
  constexpr int e0 = 4 * a + 1 - jj, e1 = 4 * a + 3 - jj;
  if constexpr (e0 >= 0) st.acc[c][0] = mfma16<F16>(st.A[e0 >= 0 ? e0 : 0], st.B[F % 3], jj == 0 ? (f32x4){0.f, 0.f, 0.f, 0.f} : st.acc[c][0]);
  st.acc[c][1] = mfma16<F16>(st.A[e1], st.B[F % 3], jj == 0 ? (f32x4){0.f, 0.f, 0.f, 0.f} : st.acc[c][1]);
  if constexpr (a > 0 && rel == 1) mrg2(st);
  if constexpr (a > 0 && rel == 2) { mrg3<(a > 0 ? 2 * a - 1 : 0), F16>(st, yp, oscale); signal_nb(cy + 4 * (a - 1)); STAMPW(0, 2 + a); }
  if constexpr (rel == nrel - 6) mrg1<0>(st);
  if constexpr (rel == nrel - 4) mrg2(st);
  if constexpr (rel == nrel - 2) mrg3<2 * a, F16>(st, yp, oscale);
  __builtin_amdgcn_sched_barrier(0);
  if constexpr (F + 1 < 160) conv_step<F16, STREAMED, (F + 1 < 160 ? F + 1 : 0)>(st, xb, ab, yp, cx, cy, lane, oscale, ws);
}

__device__ __forceinline__ f32x2 cmul(f32x2 a, f32x2 b) {
  f32x2 r;
  asm("v_pk_mul_f32 %0, %1, %2 op_sel:[0,0] op_sel_hi:[0,1]\n\t"
      "v_pk_fma_f32 %0, %1, %2, %0 op_sel:[1,1,0] op_sel_hi:[1,0,1] neg_lo:[1,0,0]" : "=&v"(r) : "v"(a), "v"(b));
  return r;
}
__device__ __forceinline__ f32x2 cpow(float al, float rh, float rl, float m) {
  const float e = __builtin_amdgcn_exp2f(al * m);
  const float f = __builtin_fmaf(rl, m, __builtin_amdgcn_fractf(rh * m));
  f32x2 z = {e * __builtin_amdgcn_cosf(f), e * __builtin_amdgcn_sinf(f)};
  return z;
}
__device__ __forceinline__ void pow2_scale(u32 mbits, int T, float& s, float& inv) {
  int e = (int)((mbits >> 23) & 0xff);
  e = e < 1 ? 1 : (e > 254 ? 254 : e);
  int se = 254 + T - e;
  se = se < 1 ? 1 : (se > 253 ? 253 : se);
  s = __uint_as_float((u32)se << 23);
  inv = __uint_as_float((u32)(254 - se) << 23);
}

struct Params {
  const u16 *q, *k, *v, *shk, *ssmk, *ssmD, *u, *Are, *Aim, *Bre, *Bim, *Cre, *Cim, *logdt, *Dskip;
  u16* out;
  char* ws;
};

template <bool F16, bool STREAMED, int X2>
__device__ __forceinline__ void conv_wave(lptr hs, lptr xs, lptr cx, lptr cy, int lane, float oscale, float dfold, char* ws) {
  Stream st;
  st.sv = 0.f;
  const int kg = lane >> 4, q = (lane & 15) >> 2, p = lane & 3;
  lptr ab = hs + (256 - 64 * kg - 8 * q + 8 * p);
  lptr xb = xs + (8 + 16 * kg + 8 * (lane & 15)) + ((lane & 1) ? 0 : X2);
  lptr yp = hs + (kg >> 1) * 512 + (kg & 1) * 128 + 8 * (lane & 15);
  if (STREAMED) {
    if (lane == 0) *(LDS_AS u16*)(hs + 32) = (u16)pk_bf16(bf_lo(*(LDS_AS u16*)(hs + 32)) + dfold, 0.f);
    CFENCE();
  }
  load_afrag<1>(st, ab); load_afrag<3>(st, ab); load_afrag<0>(st, ab); load_afrag<2>(st, ab);
  if (STREAMED) { wait_cnt(cx, 4); __builtin_amdgcn_s_setprio(2); }
  STAMPW(0, 2);
  st.B[0] = *(volatile LDS_AS s16x8*)(xb + frag_off(0));
  st.B[1] = *(volatile LDS_AS s16x8*)(xb + frag_off(1));
  conv_step<F16, STREAMED, 0>(st, xb, ab, yp, cx, cy, lane, oscale, ws);
  asm volatile("s_nop 7" : "+v"(st.acc[3][1]));
  mrg1<1>(st); mrg2(st); mrg3<7, F16>(st, yp, oscale);
  signal_nb(cy + 12);
  STAMPW(0, 6);
}

constexpr int RAW_U = 2112;
constexpr int A_RAW = 0;
constexpr int A_XS = A_RAW + 16 * RAW_U;
constexpr int A_HS = A_XS + XS_ALL;
constexpr int A_CNT = A_HS + 8 * HS_CH;
constexpr int A_END = A_CNT + 64;

template <int DC>
__device__ __forceinline__ void kv_block(lptr sm, lptr raw, int s, int lane, s16x8 W0, s16x8 W1) {
  const int r = lane & 15, kg = lane >> 4;
  lptr ap = raw + (8 * (r >> 2) + (r & 3) + 4 - kg) * 16;
  s16x8 a00 = *(LDS_AS s16x8*)(ap), a01 = *(LDS_AS s16x8*)(ap + 512);
  s16x8 a10 = *(LDS_AS s16x8*)(ap + 64), a11 = *(LDS_AS s16x8*)(ap + 64 + 512);
  const int q = (lane & 15) >> 2, p = lane & 3;
  lptr vp = raw + 1088 + (8 * (kg + 4 * (p >> 1)) + q) * 16 + 8 * (p & 1);
  u32x2 V0 = tr_read(vp), V1 = tr_read(vp + 64);
  f32x4 d0 = mfma16<false>(a00, W0, (f32x4){0.f, 0.f, 0.f, 0.f});
  f32x4 d1 = mfma16<false>(a10, W0, (f32x4){0.f, 0.f, 0.f, 0.f});
  d0 = mfma16<false>(a01, W1, d0);
  d1 = mfma16<false>(a11, W1, d1);
  const float v0[4] = {bf_lo(V0.x), bf_hi(V0.x), bf_lo(V0.y), bf_hi(V0.y)};
  const float v1[4] = {bf_lo(V1.x), bf_hi(V1.x), bf_lo(V1.y), bf_hi(V1.y)};
  const int ch = lane & 7, ip = kg + 4 * ((lane >> 3) & 1);
  lptr dst = sm + A_XS + ch * XS_CH + 128 + (128 * DC + 16 * s + 2 * ip) * 2;
#pragma unroll
  for (int c = 0; c < 4; ++c) {
    float a = d0[c], b = d1[c];
    rbf2(a, b);
    const u32 w = pk_bf16(fmul(a, v0[c]), fmul(b, v1[c]));
    *(LDS_AS u32*)(dst + c * XP) = w;
    *(LDS_AS u32*)(dst + c * XP + XS2) = w;
  }
}

template <int DT>
__device__ __forceinline__ void final_A(lptr sm, int s, int lane, u32x4 qv, u16* outrow0) {
  const int c = lane >> 4, q = (lane & 15) >> 2, p = lane & 3;
  const int yoff = q * HS_CH + 1024 * DT + (c >> 1) * 512 + (s >> 2) * 256 + (c & 1) * 128 + (s & 3) * 32 + 8 * p;
  u32x2 Y0 = tr_read(sm + A_HS + yoff), Y1 = tr_read(sm + A_HS + yoff + 4 * HS_CH);
  u32 yv[4] = {Y0.x, Y0.y, Y1.x, Y1.y};
  u32x4 o;
#pragma unroll
  for (int e = 0; e < 4; ++e) o[e] = pk_bf16(fmul(bf_lo(yv[e]), bf_lo(qv[e])), fmul(bf_hi(yv[e]), bf_hi(qv[e])));
  u16* dst = outrow0 + (size_t)(512 * DT) * 1280;
  asm volatile("global_store_dwordx4 %0, %1, off\n\ts_nop 1" :: "v"(dst), "v"(o) : "memory");
}

__device__ __forceinline__ void head_A(const Params& P, lptr sm, int ch0, char* ws) {
  const int tid = threadIdx.x, lane = tid & 63;
  const int wave = __builtin_amdgcn_readfirstlane(tid >> 6);
  const u32 lb = (u32)(size_t)sm;
  lptr cx = sm + A_CNT, cy = sm + A_CNT + 32;
  if (tid < 4) *(LDS_AS u32x4*)(sm + A_CNT + tid * 16) = (u32x4){0, 0, 0, 0};
  if (wave < 8) {
    u32 dv;
    asm volatile("global_load_ushort %0, %1, off" : "=v"(dv) : "v"(P.ssmD + ch0 + wave) : "memory");
    dma16((const char*)(P.ssmk + (size_t)(ch0 + wave) * S) + lane * 16, lb + A_HS + wave * HS_CH + 32);
    BAR();
    const u32x4 z = {0, 0, 0, 0};
    if (lane < 32) *(LDS_AS u32x4*)(sm + A_XS + wave * XS_CH + (lane >> 3) * XP + (lane & 7) * 16) = z;
    *(LDS_AS u32x2*)(sm + A_XS + XS2 + wave * XS_CH + (lane >> 4) * XP + (lane & 15) * 8) = (u32x2){0, 0};
    if (lane < 2) *(LDS_AS u32x4*)(sm + A_HS + wave * HS_CH + lane * 16) = z;
    WAITV(0);
    asm volatile("" : "+v"(dv));
#pragma unroll
    for (int i = 1; i < 4; ++i)
      dma16((const char*)(P.ssmk + (size_t)(ch0 + wave) * S) + i * 1024 + lane * 16, lb + A_HS + wave * HS_CH + 32 + i * 1024);
    conv_wave<false, true, XS2>(sm + A_HS + wave * HS_CH, sm + A_XS + wave * XS_CH, cx, cy, lane, 1.0f, bf_lo(dv), ws);
  } else {
    __builtin_amdgcn_s_setprio(3);
    const int s = wave - 8;
    const char* kp = (const char*)(P.k + ch0) + (size_t)(s * 64 + lane) * 1280;
    const char* vp = (const char*)(P.v + ch0) + (size_t)(s * 64 + lane) * 1280;
    const u32 rawb = lb + A_RAW + s * 2 * RAW_U;
    lptr raw0 = sm + A_RAW + s * 2 * RAW_U, raw1 = raw0 + RAW_U;
#define ISSUE(d) do {                                                                                    \
      if ((d) > 0 || s > 0) { if (lane < 4) dma16(kp + ((size_t)(d) * 512 - 4) * 1280, rawb + ((d) & 1) * RAW_U); } \
      else if (lane < 4) *(LDS_AS u32x4*)(raw0 + lane * 16) = (u32x4){0, 0, 0, 0};                       \
      dma16(kp + (size_t)(d) * 512 * 1280, rawb + ((d) & 1) * RAW_U + 64);                               \
      dma16(vp + (size_t)(d) * 512 * 1280, rawb + ((d) & 1) * RAW_U + 1088);                             \
    } while (0)
    u32 wt;
    asm volatile("global_load_ushort %0, %1, off" : "=v"(wt) : "v"(P.shk + (ch0 + (lane & 7)) * 4 + (lane >> 4)) : "memory");
    BAR();
    __builtin_amdgcn_s_sleep(D0DELAY);
    ISSUE(0);
    signal_nb(cx + 48);
    wait_cnt(cx + 48, 8);
    ISSUE(1);
    const int frow = 4 * (16 * s + (lane & 15)) + (lane >> 4);
    const char* qp = (const char*)(P.q + ch0) + (size_t)frow * 1280;
    u16* op = P.out + (size_t)frow * 1280 + ch0;
    u32x4 qa, qb, qc, qd;
#define QLOAD(Q, dt) asm volatile("global_load_dwordx4 %0, %1, off" KVPOL : "=v"(Q) : "v"(qp + (size_t)(dt) * 512 * 1280) : "memory")
    WAITV(5);
    asm volatile("" : "+v"(wt));
    s16x8 W0, W1;
    {
      const int ch = lane & 7;
      const u32 wv = wt << (16 * (ch & 1));
      u32x4 w = {(ch >> 1) == 0 ? wv : 0u, (ch >> 1) == 1 ? wv : 0u, (ch >> 1) == 2 ? wv : 0u, (ch >> 1) == 3 ? wv : 0u};
      const u32x4 z = {0, 0, 0, 0};
      W0 = __builtin_bit_cast(s16x8, (lane & 8) ? z : w);
      W1 = __builtin_bit_cast(s16x8, (lane & 8) ? w : z);
    }
    lptr cxs = cx + (s >> 2) * 4;
    asm volatile("" : "+v"(W0), "+v"(W1));
    WAITV(3);
    STAMPW(15, 1);
    kv_block<0>(sm, raw0, s, lane, W0, W1);
    signal_nb(cxs + 0);
    STAMPW(15, 2);
    WAITV(0);
    kv_block<1>(sm, raw1, s, lane, W0, W1);
    signal_nb(cxs + 8);
    asm volatile("s_waitcnt lgkmcnt(0)" ::: "memory");
    STAMPW(15, 3);
    wait_cnt(cx + 8, 4); wait_cnt(cx + 12, 4);
    ISSUE(2);
    QLOAD(qa, 0);
    WAITV(1);
    kv_block<2>(sm, raw0, s, lane, W0, W1);
    signal_nb(cxs + 16);
    wait_cnt(cx + 16, 4); wait_cnt(cx + 20, 4);
    ISSUE(3);
    QLOAD(qb, 1);
    wait_cnt(cy + 0, 8);
    WAITV(4);
    asm volatile("" : "+v"(qa));
    final_A<0>(sm, s, lane, qa, op);
    WAITV(2);
    kv_block<3>(sm, raw1, s, lane, W0, W1);
    signal_nb(cxs + 24);
    STAMPW(15, 4);
    wait_cnt(cx + 24, 4); wait_cnt(cx + 28, 4);
    QLOAD(qc, 2);
    QLOAD(qd, 3);
    wait_cnt(cy + 4, 8);
    WAITV(3);
    asm volatile("" : "+v"(qb));
    final_A<1>(sm, s, lane, qb, op);
    wait_cnt(cy + 8, 8);
    WAITV(1);
    asm volatile("" : "+v"(qc), "+v"(qd));
    final_A<2>(sm, s, lane, qc, op);
    STAMPW(15, 5);
    wait_cnt(cy + 12, 8);
    final_A<3>(sm, s, lane, qd, op);
#undef ISSUE
#undef QLOAD
  }
}

struct Pref { int l0, nl, part, nparts; u32 scratch; };
template <int E0, int E1, int ROUNDS>
__device__ __forceinline__ void prefetch(const Params& P, const Pref& pf, int lane) {
#pragma unroll
  for (int r = 0; r < ROUNDS; ++r) {
    int wi = r * pf.nparts + pf.part;
    const int sh = pf.nl - 1;
    if (wi >= ((E1 - E0) << (3 + sh))) { if (E1 < 12) return; wi = 0; }
    const int e = E0 + (wi >> (3 + sh));
    const int rem = ((wi & ((8 << sh) - 1)) << 6) + lane;
    const int ty = (0x222102101010ull >> (4 * e)) & 3, dc = (0x321330221100ull >> (4 * e)) & 3;
    const u16* base = ty == 0 ? P.k : (ty == 1 ? P.v : P.q);
    dma4((const char*)base + (size_t)(dc * 512 + (rem >> sh)) * 1280 + (pf.l0 + (rem & sh)) * 128, pf.scratch);
  }
}

constexpr int B_XS2 = 4 * XS_CH + 8;
constexpr int B_RAW = 0;
constexpr int B_XS = 16384;
constexpr int B_TB = B_XS + 8 * XS_CH + 256;
constexpr int B_TST = 4352;
constexpr int B_HS = B_TB + 8 * B_TST;
constexpr int B_KP = B_HS + 4 * HS_CH;
constexpr int B_CNT = B_KP + 4 * 1312;
constexpr int B_END = B_CNT + 64;

template <int DT>
__device__ __forceinline__ void final_B(lptr sm, int s, int lane, u16* outrow0) {
  const int c = lane >> 4, q = (lane & 15) >> 2, p = lane & 3;
  const int yoff = q * HS_CH + 1024 * DT + (c >> 1) * 512 + (s >> 2) * 256 + (c & 1) * 128 + (s & 3) * 32 + 8 * p;
  u32x2 o = tr_read(sm + B_HS + yoff);
  u16* dst = outrow0 + (size_t)(512 * DT) * 1280;
  asm volatile("global_store_dwordx2 %0, %1, off\n\ts_nop 1" :: "v"(dst), "v"(o) : "memory");
}

__device__ __forceinline__ void head_B(const Params& P, lptr sm, int ch0, int g, Pref pf, char* ws) {
  const int tid = threadIdx.x, lane = tid & 63;
  const int wave = __builtin_amdgcn_readfirstlane(tid >> 6);
  const u32 lb = (u32)(size_t)sm;
  lptr cnt = sm + B_CNT, cy = sm + B_CNT + 32;
  if (tid < 4) *(LDS_AS u32x4*)(sm + B_CNT + tid * 16) = (u32x4){0, 0, 0, 0};
  if (wave >= 8) {
    const int s = wave - 8;
    pf.part = s * pf.nparts + pf.part; pf.nparts *= 8; pf.scratch += s * 256;
    prefetch<0, 2, 1>(P, pf, lane);
    BAR();
    __builtin_amdgcn_s_sleep(D1DELAY);
    prefetch<2, 4, 1>(P, pf, lane);
    __builtin_amdgcn_s_sleep(UDELAY);
    __builtin_amdgcn_s_setprio(3);
    {
      u32x2 r[4];
#pragma unroll
      for (int i = 0; i < 4; ++i) {
        const int rb = 256 * s + 64 * ((i + g) & 3);
        asm volatile("global_load_dwordx2 %0, %1, off" UPOL : "=v"(r[i]) : "v"((const char*)(P.u + ch0) + (size_t)(rb + lane) * 1280) : "memory");
      }
      prefetch<4, 12, 1>(P, pf, lane);
      WAITV(1);
      asm volatile("" : "+v"(r[0]), "+v"(r[1]), "+v"(r[2]), "+v"(r[3]));
#pragma unroll
      for (int i = 0; i < 4; ++i) *(LDS_AS u32x2*)(sm + B_RAW + (256 * s + 64 * ((i + g) & 3) + lane) * 8) = r[i];
      CFENCE();
    }
    STAMPW(15, 1);
    u32x2 U[4];
    {
      const int gq = lane >> 4, q = (lane & 15) >> 2, p = lane & 3;
      lptr rp = sm + B_RAW + (256 * s + 8 * (4 * gq + p) + q) * 8;
      U[0] = tr_read(rp); U[1] = tr_read(rp + 32); U[2] = tr_read(rp + 1024); U[3] = tr_read(rp + 1024 + 32);
      u16x2 mx = {0, 0};
#pragma unroll
      for (int i = 0; i < 4; ++i) {
        mx = __builtin_elementwise_max(mx, __builtin_bit_cast(u16x2, U[i].x & 0x7fff7fffu));
        mx = __builtin_elementwise_max(mx, __builtin_bit_cast(u16x2, U[i].y & 0x7fff7fffu));
      }
      u32 m1 = max((u32)mx.x, (u32)mx.y);
      m1 = max(m1, (u32)__builtin_amdgcn_update_dpp(0u, m1, 0x124, 0xf, 0xf, false));
      m1 = max(m1, (u32)__builtin_amdgcn_update_dpp(0u, m1, 0x128, 0xf, 0xf, false));
      if ((lane & 15) < 4) __hip_atomic_fetch_max((LDS_AS u32*)(cnt + (lane & 3) * 4), m1, __ATOMIC_RELAXED, __HIP_MEMORY_SCOPE_WORKGROUP);
    }
    signal_nb(cnt + 16);
    __builtin_amdgcn_s_setprio(0);
    wait_cnt(cnt + 16, 8);
    {
      const int ch = lane & 3, ip = lane >> 2;
      float us, uinv;
      pow2_scale(*(volatile LDS_AS u32*)(cnt + ch * 4) << 16, 12, us, uinv);
      lptr dst = sm + B_XS + ch * XS_CH + 128 + (64 * s + 2 * ip) * 2;
#pragma unroll
      for (int h = 0; h < 2; ++h) {
        u32x2 a = U[2 * h], b = U[2 * h + 1];
        u32 w[4] = {pk_f16(fmul(bf_lo(a.x), us), fmul(bf_lo(b.x), us)), pk_f16(fmul(bf_hi(a.x), us), fmul(bf_hi(b.x), us)),
                    pk_f16(fmul(bf_lo(a.y), us), fmul(bf_lo(b.y), us)), pk_f16(fmul(bf_hi(a.y), us), fmul(bf_hi(b.y), us))};
#pragma unroll
        for (int c = 0; c < 4; ++c) {
          *(LDS_AS u32*)(dst + h * 64 + c * XP) = w[c];
          *(LDS_AS u32*)(dst + h * 64 + c * XP + B_XS2) = w[c];
        }
      }
    }
    signal_nb(cnt + 20);
    STAMPW(15, 2);
    const int frow = 4 * (16 * s + (lane & 15)) + (lane >> 4);
    u16* op = P.out + (size_t)frow * 1280 + 640 + ch0;
    wait_cnt(cy + 0, 4);
    final_B<0>(sm, s, lane, op);
    wait_cnt(cy + 4, 4);
    final_B<1>(sm, s, lane, op);
    wait_cnt(cy + 8, 4);
    final_B<2>(sm, s, lane, op);
    wait_cnt(cy + 12, 4);
    final_B<3>(sm, s, lane, op);
    return;
  }
  const int w4 = wave & 3, half = wave >> 2;
  const int d = ch0 + w4;
  u32 pr[6];
  u32 cw, ldt;
  {
    const size_t po = (size_t)d * NM * 2 + lane * 4;
    asm volatile("global_load_ushort %0, %1, off" : "=v"(ldt) : "v"(P.logdt + d) : "memory");
    asm volatile("global_load_dword %0, %1, off" : "=v"(pr[0]) : "v"((const char*)P.Are + po) : "memory");
    asm volatile("global_load_dword %0, %1, off" : "=v"(pr[1]) : "v"((const char*)P.Aim + po) : "memory");
    asm volatile("global_load_dword %0, %1, off" : "=v"(pr[2]) : "v"((const char*)P.Bre + po) : "memory");
    asm volatile("global_load_dword %0, %1, off" : "=v"(pr[3]) : "v"((const char*)P.Bim + po) : "memory");
    asm volatile("global_load_dword %0, %1, off" : "=v"(pr[4]) : "v"((const char*)P.Cre + po) : "memory");
    asm volatile("global_load_dword %0, %1, off" : "=v"(pr[5]) : "v"((const char*)P.Cim + po) : "memory");
    asm volatile("global_load_ushort %0, %1, off" : "=v"(cw) : "v"(P.Dskip + d) : "memory");
  }
  BAR();
  if (!half) {
    lptr hs = sm + B_HS + wave * HS_CH;
    if (lane < 2) *(LDS_AS u32x4*)(hs + lane * 16) = (u32x4){0, 0, 0, 0};
    if (lane < 32) *(LDS_AS u32x4*)(sm + B_XS + wave * XS_CH + (lane >> 3) * XP + (lane & 7) * 16) = (u32x4){0, 0, 0, 0};
    *(LDS_AS u32x2*)(sm + B_XS + B_XS2 + wave * XS_CH + (lane >> 4) * XP + (lane & 15) * 8) = (u32x2){0, 0};
  }
  WAITV(0);
  asm volatile("" : "+v"(pr[0]), "+v"(pr[1]), "+v"(pr[2]), "+v"(pr[3]), "+v"(pr[4]), "+v"(pr[5]), "+v"(ldt), "+v"(cw));
  { char* ws2 = ws + 8 * 8 * 256; char* ws = ws2; STAMPW(0, 3); }
  lptr tb = sm + B_TB + wave * B_TST;
  constexpr int RS = 272;
  const float dt = rbf(expf(bf_lo(ldt)));
  float cs, cinv;
  f32x2 G[8];
  float al, rh, rl;
  {
    const float aa = dt * (half ? bf_hi(pr[0]) : bf_lo(pr[0]));
    const float bb = dt * (half ? bf_hi(pr[1]) : bf_lo(pr[1]));
    al = aa * 1.44269504088896341f;
    const float I2PH = 0.15915494309189535f, I2PL = 6.4206383e-09f;
    rh = bb * I2PH;
    rl = __builtin_fmaf(bb, I2PL, __builtin_fmaf(bb, I2PH, -rh));
  }
  lptr tw = tb + lane * 4;
  lptr trd = tb + (lane & 15) * RS + (lane >> 4) * 16;
  {
    const f32x2 z8 = cpow(al, -rh, -rl, 8.f);
    const f32x2 z16 = cmul(z8, z8);
    f32x2 fo = z8, fe = z16;
    *(LDS_AS u32*)(tw) = pk_f16(1.f, 0.f);
#pragma unroll
    for (int j = 1; j < 16; j += 2) {
      *(LDS_AS u32*)(tw + j * RS) = pk_f16v(fo);
      if (j < 15) {
        *(LDS_AS u32*)(tw + (j + 1) * RS) = pk_f16v(fe);
        fo = cmul(fo, z16);
        if (j < 13) fe = cmul(fe, z16);
      }
    }
  }
  {
    f32x2 c0 = cmul((f32x2){bf_lo(pr[4]), bf_lo(pr[5])}, (f32x2){dt * bf_lo(pr[2]), dt * bf_lo(pr[3])});
    f32x2 c1 = cmul((f32x2){bf_hi(pr[4]), bf_hi(pr[5])}, (f32x2){dt * bf_hi(pr[2]), dt * bf_hi(pr[3])});
    float sb = fabsf(c0.x) + fabsf(c0.y) + fabsf(c1.x) + fabsf(c1.y);
#pragma unroll
    for (int o = 32; o >= 1; o >>= 1) sb += __shfl_xor(sb, o);
    sb += fabsf(bf_lo(cw));
    pow2_scale(__float_as_uint(sb), 14, cs, cinv);
    G[0] = half ? c1 : c0;
    G[0] *= cs;
  }
  CFENCE();
  f16x8 Ff[4];
#pragma unroll
  for (int ks = 0; ks < 4; ++ks) Ff[ks] = *(LDS_AS f16x8*)(trd + ks * 64);
  CFENCE();
  f32x4 kacc[3];
  const f32x2 v1 = cpow(al, rh, rl, 128.f);
  {
    const f32x2 z1 = cpow(al, rh, rl, 1.f);
    const f32x2 z2 = cmul(z1, z1);
    G[1] = cmul(G[0], z1);
#pragma unroll
    for (int p = 2; p < 8; ++p) G[p] = cmul(G[p - 2], z2);
#pragma unroll
    for (int p = 0; p < 8; ++p) {
      *(LDS_AS u32*)(tw + p * RS) = pk_f16v(G[p]);
      *(LDS_AS u32*)(tw + (8 + p) * RS) = pk_f16v(cmul(G[p], v1));
    }
    CFENCE();
    kacc[0] = (f32x4){0.f, 0.f, 0.f, 0.f};
#pragma unroll
    for (int ks = 0; ks < 4; ++ks)
      kacc[0] = __builtin_amdgcn_mfma_f32_16x16x32_f16(*(LDS_AS f16x8*)(trd + ks * 64), Ff[ks], kacc[0], 0, 0, 0);
    CFENCE();
  }
  const f32x2 v2 = cmul(v1, v1);
  const f32x2 v4 = cpow(al, rh, rl, 512.f);
  {
#pragma unroll
    for (int p = 0; p < 4; ++p) {
      f32x2 t = cmul(G[2 * p], v2);
      *(LDS_AS u32*)(tw + p * RS) = pk_f16v(t);
      *(LDS_AS u32*)(tw + (4 + p) * RS) = pk_f16v(cmul(t, v1));
    }
#pragma unroll
    for (int p = 0; p < 2; ++p) {
      f32x2 t = cmul(G[4 * p], v4);
#pragma unroll
      for (int a = 0; a < 4; ++a) {
        *(LDS_AS u32*)(tw + (8 + 2 * a + p) * RS) = pk_f16v(t);
        if (a < 3) t = cmul(t, v1);
      }
    }
    CFENCE();
    kacc[1] = (f32x4){0.f, 0.f, 0.f, 0.f};
#pragma unroll
    for (int ks = 0; ks < 4; ++ks)
      kacc[1] = __builtin_amdgcn_mfma_f32_16x16x32_f16(*(LDS_AS f16x8*)(trd + ks * 64), Ff[ks], kacc[1], 0, 0, 0);
    CFENCE();
  }
  {
    f32x2 te = cmul(G[0], cmul(v4, v4));
    f32x2 to = cmul(te, v1);
#pragma unroll
    for (int a = 0; a < 9; a += 2) {
      *(LDS_AS u32*)(tw + a * RS) = pk_f16v(te);
      if (a < 8) {
        *(LDS_AS u32*)(tw + (a + 1) * RS) = pk_f16v(to);
        te = cmul(te, v2);
        if (a < 6) to = cmul(to, v2);
      }
    }
    CFENCE();
    kacc[2] = (f32x4){0.f, 0.f, 0.f, 0.f};
#pragma unroll
    for (int ks = 0; ks < 4; ++ks)
      kacc[2] = __builtin_amdgcn_mfma_f32_16x16x32_f16(*(LDS_AS f16x8*)(trd + ks * 64), Ff[ks], kacc[2], 0, 0, 0);
    CFENCE();
  }
  if (half) {
#pragma unroll
    for (int t = 0; t < 3; ++t) *(LDS_AS f32x4*)(tb + t * 1024 + lane * 16) = kacc[t];
    signal_nb(cnt + 48 + 4 * w4);
    return;
  }
  { char* ws2 = ws + 8 * 8 * 256; char* ws = ws2; STAMPW(0, 4); }
  wait_cnt(cnt + 48 + 4 * w4, 1);
  lptr Q = sm + B_KP + wave * 1312;
  lptr hs = sm + B_HS + wave * HS_CH;
  {
    lptr ob = sm + B_TB + (wave + 4) * B_TST;
    const int r16 = lane & 15, kg = lane >> 4;
#pragma unroll
    for (int t = 0; t < 3; ++t) {
      f32x4 o = *(LDS_AS f32x4*)(ob + t * 1024 + lane * 16);
      o += kacc[t];
      if (t == 0 && lane == 0) o[0] += bf_lo(cw) * cs;
      u32 a = pk_f16(o[0], o[1]), b = pk_f16(o[2], o[3]);
      if (t == 0) {
        u32x2 w = {a, b};
        *(LDS_AS u32x2*)(hs + 32 + 2 * (4 * (kg & 1) + 128 * (kg >> 1) + 8 * r16)) = w;
      } else if (t == 1) {
        if (kg < 2) {
          u32x2 w = {a, b};
          *(LDS_AS u32x2*)(Q + 2 * (64 * kg + 4 * r16)) = w;
        } else {
          *(LDS_AS u32*)(Q + 2 * (128 + 64 * (kg - 2) + 2 * r16)) = a;
          *(LDS_AS u32*)(Q + 2 * (128 + 64 * (kg - 2) + 32 + 2 * r16)) = b;
        }
      } else {
        if (kg < 2) {
          *(LDS_AS u16*)(Q + 2 * (256 + 64 * kg + r16)) = (u16)a;
          *(LDS_AS u16*)(Q + 2 * (256 + 64 * kg + 16 + r16)) = (u16)(a >> 16);
          *(LDS_AS u16*)(Q + 2 * (256 + 64 * kg + 32 + r16)) = (u16)b;
          *(LDS_AS u16*)(Q + 2 * (256 + 64 * kg + 48 + r16)) = (u16)(b >> 16);
        } else if (lane == 32) {
          *(LDS_AS u16*)(Q + 2 * 384) = (u16)a;
        }
      }
    }
  }
  CFENCE();
  {
    {
      u32 w = *(LDS_AS u32*)(Q + 2 * (2 * lane));
      u32 v2q = *(LDS_AS u16*)(Q + 2 * (2 * lane + 2));
      u32x2 o = {__builtin_amdgcn_perm(w, w, 0x01000100), __builtin_amdgcn_perm(v2q, w, 0x05040302)};
      *(LDS_AS u32x2*)(hs + 32 + 512 + lane * 8) = o;
    }
    {
      u32 w = *(LDS_AS u32*)(Q + 2 * (128 + 2 * lane));
      u32 v2q = *(LDS_AS u16*)(Q + 2 * (128 + 2 * lane + 2));
      u32x4 o = {__builtin_amdgcn_perm(w, w, 0x01000100), w, __builtin_amdgcn_perm(w, w, 0x03020302), __builtin_amdgcn_perm(v2q, v2q, 0x01000100)};
      *(LDS_AS u32x4*)(hs + 32 + 1024 + lane * 16) = o;
    }
    {
      u32 w = *(LDS_AS u32*)(Q + 2 * (256 + 2 * lane));
      u32 v2q = *(LDS_AS u16*)(Q + 2 * (256 + 2 * lane + 2));
      u32 a0 = __builtin_amdgcn_perm(w, w, 0x01000100), a1 = __builtin_amdgcn_perm(w, w, 0x03020302), a2 = __builtin_amdgcn_perm(v2q, v2q, 0x01000100);
      u32x4 o0 = {a0, a0, w, a1}, o1 = {a1, a1, a2, a2};
      *(LDS_AS u32x4*)(hs + 32 + 2048 + lane * 32) = o0;
      *(LDS_AS u32x4*)(hs + 32 + 2048 + lane * 32 + 16) = o1;
    }
  }
  { char* ws2 = ws + 8 * 8 * 256; char* ws = ws2; STAMPW(0, 5); }
  wait_cnt(cnt + 20, 8);
  float us2, uinv2;
  pow2_scale(*(volatile LDS_AS u32*)(cnt + wave * 4) << 16, 12, us2, uinv2);
  conv_wave<true, false, B_XS2>(hs, sm + B_XS + wave * XS_CH, cy, cy, lane, uinv2 * cinv, 0.f, ws);
}

__global__ __launch_bounds__(1024) void k_fused(Params P) {
  constexpr int SCRATCH = A_END > B_END ? A_END : B_END;
  __shared__ __attribute__((aligned(256))) char smem[SCRATCH + 2048];
  lptr sm = (lptr)smem;
  char* ws = P.ws;
  STAMPW(0, 0);
  STAMPW(15, 0);
  const int b = blockIdx.x;
  const int xcd = b & 7, slot = b >> 3;
  const int nA = xcd < 4 ? 12 : 8, nB = xcd < 4 ? 16 : 24;
  const int a0 = xcd < 4 ? 96 * xcd : 384 + 64 * (xcd - 4);
  const int b0 = xcd < 4 ? 64 * xcd : 256 + 96 * (xcd - 4);
  Pref pf;
  pf.l0 = (a0 * 2) >> 7;
  pf.nl = (((a0 + nA * 8) * 2 - 1) >> 7) - pf.l0 + 1;
  pf.part = slot - nA; pf.nparts = nB; pf.scratch = (u32)(size_t)sm + SCRATCH;
  if (slot < nA) {
    head_A(P, sm, a0 + slot * 8, ws);
  } else if (slot < nA + nB) {
    head_B(P, sm, b0 + (slot - nA) * 4, slot, pf, ws);
  }
}

extern "C" void kernel_launch(void* const* d_in, const int* in_sizes, int n_in,
                              void* d_out, int out_size, void* d_ws, size_t ws_size,
                              hipStream_t stream) {
  Params P;
  P.q = (const u16*)d_in[0]; P.k = (const u16*)d_in[1]; P.v = (const u16*)d_in[2];
  P.shk = (const u16*)d_in[3]; P.ssmk = (const u16*)d_in[4]; P.ssmD = (const u16*)d_in[5];
  P.u = (const u16*)d_in[6]; P.Are = (const u16*)d_in[7]; P.Aim = (const u16*)d_in[8];
  P.Bre = (const u16*)d_in[9]; P.Bim = (const u16*)d_in[10]; P.Cre = (const u16*)d_in[11];
  P.Cim = (const u16*)d_in[12]; P.logdt = (const u16*)d_in[13]; P.Dskip = (const u16*)d_in[14];
  P.out = (u16*)d_out; P.ws = (char*)d_ws;
  k_fused<<<256, 1024, 0, stream>>>(P);
}
```

```cpp
#include <hip/hip_runtime.h>
#include <hip/hip_bf16.h>

typedef unsigned int u32;
typedef unsigned short u16;
typedef __attribute__((ext_vector_type(2))) u32 u32x2;
typedef __attribute__((ext_vector_type(4))) u32 u32x4;
typedef __attribute__((ext_vector_type(4))) short s16x4;
typedef __attribute__((ext_vector_type(8))) short s16x8;
typedef __attribute__((ext_vector_type(8))) __bf16 bf16x8;
typedef __attribute__((ext_vector_type(8))) _Float16 f16x8;
typedef __attribute__((ext_vector_type(2))) __bf16 bf16x2;
typedef __attribute__((ext_vector_type(2))) _Float16 f16x2;
typedef __attribute__((ext_vector_type(2))) unsigned short u16x2;
typedef __attribute__((ext_vector_type(2))) float f32x2;
typedef __attribute__((ext_vector_type(4))) float f32x4;
#define LDS_AS __attribute__((address_space(3)))
typedef LDS_AS char* lptr;

#define STAMPW(w, idx)

#define SLEEPN 1
#define EXACT_ROUND 1

constexpr int S = 2048;
constexpr int D = 640;
constexpr int NM = 128;

__device__ __forceinline__ float bf_lo(u32 v) { return __uint_as_float(v << 16); }
__device__ __forceinline__ float bf_hi(u32 v) { return __uint_as_float(v & 0xffff0000u); }
__device__ __forceinline__ u32 pk_bf16(float a, float b) {
  f32x2 v = {a, b};
  return __builtin_bit_cast(u32, __builtin_convertvector(v, bf16x2));
}
__device__ __forceinline__ u32 pk_f16(float a, float b) {
  f32x2 v = {a, b};
  return __builtin_bit_cast(u32, __builtin_convertvector(v, f16x2));
}
__device__ __forceinline__ u32 pk_f16v(f32x2 v) { return __builtin_bit_cast(u32, __builtin_convertvector(v, f16x2)); }
__device__ __forceinline__ float rbf(float a) { return bf_lo(pk_bf16(a, a)); }
__device__ __forceinline__ void rbf2(float& a, float& b) { u32 p = pk_bf16(a, b); a = bf_lo(p); b = bf_hi(p); }

#define WAITV(n) asm volatile("s_waitcnt vmcnt(" #n ")" ::: "memory")
#define BAR() do { asm volatile("s_waitcnt lgkmcnt(0)" ::: "memory"); __builtin_amdgcn_s_barrier(); asm volatile("" ::: "memory"); } while (0)
#define CFENCE() asm volatile("" ::: "memory")

__device__ __forceinline__ void dma16(const void* g, u32 lds) {
  u32 keep;
  lds = __builtin_amdgcn_readfirstlane(lds);
  asm volatile("s_mov_b32 %0, m0\n\ts_mov_b32 m0, %2\n\ts_nop 0\n\tglobal_load_lds_dwordx4 %1, off nt\n\ts_mov_b32 m0, %0"
               : "=&s"(keep) : "v"(g), "s"(lds) : "memory");
}
__device__ __forceinline__ u32x2 tr_read(lptr p) {
  return __builtin_bit_cast(u32x2, __builtin_amdgcn_ds_read_tr16_b64_v4i16((LDS_AS s16x4*)p));
}
__device__ __forceinline__ void wait_cnt(lptr p, u32 target) {
  CFENCE();
  while ((u32)__builtin_amdgcn_readfirstlane(*(volatile LDS_AS u32*)p) < target) __builtin_amdgcn_s_sleep(SLEEPN);
  CFENCE();
}
__device__ __forceinline__ void signal(lptr p, int lane) {
  CFENCE();
  if (lane == 0) __hip_atomic_fetch_add((LDS_AS u32*)p, 1u, __ATOMIC_RELAXED, __HIP_MEMORY_SCOPE_WORKGROUP);
  CFENCE();
}

__device__ __forceinline__ void signal_nb(lptr p) {
  asm volatile("s_mov_b64 exec, 1\n\tds_add_u32 %0, %1\n\ts_mov_b64 exec, -1" :: "v"((u32)(size_t)p), "v"(1u) : "memory");
}

constexpr int XP = 1152;
constexpr int XS_CH = 4640;
constexpr int XS2 = 8 * XS_CH + 128 + 8;
constexpr int XS_ALL = 2 * 8 * XS_CH + 256;
constexpr int HS_CH = 4128;

template <bool F16>
__device__ __forceinline__ f32x4 mfma16(s16x8 a, s16x8 b, f32x4 c) {
  if (F16) return __builtin_amdgcn_mfma_f32_16x16x32_f16(__builtin_bit_cast(f16x8, a), __builtin_bit_cast(f16x8, b), c, 0, 0, 0);
  else return __builtin_amdgcn_mfma_f32_16x16x32_bf16(__builtin_bit_cast(bf16x8, a), __builtin_bit_cast(bf16x8, b), c, 0, 0, 0);
}

__device__ __forceinline__ float fadd(float a, float b) { float r; asm("v_add_f32 %0, %1, %2" : "=v"(r) : "v"(a), "v"(b)); return r; }
__device__ __forceinline__ float fmul(float a, float b) { float r; asm("v_mul_f32 %0, %1, %2" : "=v"(r) : "v"(a), "v"(b)); return r; }
__device__ __forceinline__ void sw16(float& a, float& b) {
  auto r = __builtin_amdgcn_permlane16_swap(__float_as_uint(a), __float_as_uint(b), false, false);
  a = __uint_as_float(r[0]); b = __uint_as_float(r[1]);
}
__device__ __forceinline__ void sw32(float& a, float& b) {
  auto r = __builtin_amdgcn_permlane32_swap(__float_as_uint(a), __float_as_uint(b), false, false);
  a = __uint_as_float(r[0]); b = __uint_as_float(r[1]);
}
__device__ __forceinline__ void tr4(float& a, float& b, float& c, float& d) { sw32(a, c); sw32(b, d); sw16(a, b); sw16(c, d); }

struct Stream {
  s16x8 A[16];
  f32x4 acc[4][2];
  s16x8 B[3];
  float c0, c1, c2, c3, l0, l1, l2, l3, sv;
  u32 cnt;
};
constexpr int pair_of(int F) { return F < 16 ? 0 : (F < 48 ? 1 : (F < 96 ? 2 : 3)); }
constexpr int pair_base(int a) { return a == 0 ? 0 : (a == 1 ? 16 : (a == 2 ? 48 : (a == 3 ? 96 : 160))); }
constexpr int frag_off(int F) { return ((F - pair_base(pair_of(F))) & 3) * XP + 64 * ((F - pair_base(pair_of(F))) >> 2); }
constexpr int first_use(int ch) { return pair_base(ch >> 1) + 8 * ch; }
constexpr int chunk_first_used_at(int F) {
  for (int ch = 1; ch < 8; ++ch) if (first_use(ch) == F) return ch;
  return -1;
}
template <int r> __device__ __forceinline__ void mrg1(Stream& st) {
  st.c0 = fadd(fadd(st.acc[1][r][3], st.acc[2][r][2]), st.acc[3][r][1]);
  st.c1 = fadd(st.acc[2][r][3], st.acc[3][r][2]);
  st.c2 = st.acc[3][r][3];
  st.l0 = st.acc[0][r][0];
  st.l1 = fadd(st.acc[0][r][1], st.acc[1][r][0]);
  st.l2 = fadd(fadd(st.acc[0][r][2], st.acc[1][r][1]), st.acc[2][r][0]);
  st.l3 = fadd(fadd(st.acc[0][r][3], st.acc[1][r][2]), fadd(st.acc[2][r][1], st.acc[3][r][0]));
}
__device__ __forceinline__ void mrg2(Stream& st) {
  st.c3 = 0.f;
  tr4(st.l0, st.l1, st.l2, st.l3);
  tr4(st.c0, st.c1, st.c2, st.c3);
}
template <int T, bool SC> __device__ __forceinline__ void mrg3(Stream& st, lptr yp, float oscale) {
  float prev = __uint_as_float(__builtin_amdgcn_update_dpp(__float_as_uint(st.sv), __float_as_uint(st.c3), 0x111, 0xf, 0xf, false));
  st.sv = __uint_as_float(__builtin_amdgcn_update_dpp(0u, __float_as_uint(st.c3), 0x121, 0xf, 0xf, false));
  float y0 = fadd(st.l0, prev), y1 = fadd(st.l1, st.c0), y2 = fadd(st.l2, st.c1), y3 = fadd(st.l3, st.c2);
  if (SC) { y0 = fmul(y0, oscale); y1 = fmul(y1, oscale); y2 = fmul(y2, oscale); y3 = fmul(y3, oscale); }
  u32x2 w = {pk_bf16(y0, y1), pk_bf16(y2, y3)};
  *(LDS_AS u32x2*)(yp + 1024 * (T >> 1) + 256 * (T & 1)) = w;
}
template <int E> __device__ __forceinline__ void load_afrag(Stream& st, lptr ab) {
  s16x4 lo = __builtin_amdgcn_ds_read_tr16_b64_v4i16((LDS_AS s16x4*)(ab + 256 * E));
  s16x4 hi = __builtin_amdgcn_ds_read_tr16_b64_v4i16((LDS_AS s16x4*)(ab + 256 * E - 32));
  st.A[E] = __builtin_shufflevector(lo, hi, 0, 1, 2, 3, 4, 5, 6, 7);
}
#define PIN_ACC(st) asm volatile("" : "+v"(st.acc[0][0]), "+v"(st.acc[1][0]), "+v"(st.acc[2][0]), "+v"(st.acc[3][0]), \
                                      "+v"(st.acc[0][1]), "+v"(st.acc[1][1]), "+v"(st.acc[2][1]), "+v"(st.acc[3][1]))
template <bool F16, bool STREAMED, int F>
__device__ __forceinline__ void conv_step(Stream& st, lptr xb, lptr ab, lptr yp, lptr cx, lptr cy, int lane, float oscale, char* ws) {
  constexpr int PD = 2;
  constexpr int a = pair_of(F), rel = F - pair_base(a), jj = rel >> 2, c = rel & 3;
  constexpr int nrel = pair_base(a + 1) - pair_base(a);
  if constexpr (STREAMED) {
    constexpr int chE = chunk_first_used_at(F + PD + 3), chN = chunk_first_used_at(F + PD);
    if constexpr (chE > 0) st.cnt = *(volatile LDS_AS u32*)(cx + 4 * (chE > 0 ? chE : 0));
    if constexpr (chN > 0) {
      if ((u32)__builtin_amdgcn_readfirstlane(st.cnt) < 4u) {
        if constexpr (F >= 4) PIN_ACC(st);
        wait_cnt(cx + 4 * (chN > 0 ? chN : 0), 4);
      }
      CFENCE();
    }
    if constexpr (a < 3 && rel == 4) { if (a == 0) WAITV(2); else if (a == 1) WAITV(1); else WAITV(0); }
  }
  if constexpr (F + PD < 160) st.B[(F + PD) % 3] = *(volatile LDS_AS s16x8*)(xb + frag_off(F + PD));
  if constexpr (a < 3 && rel >= 4 && rel < 8) load_afrag<(a < 3 ? 4 * (a + 1) + (rel & 3) : 0)>(st, ab);
  if constexpr (a > 0 && rel == 0) { asm volatile("s_nop 6" : "+v"(st.acc[3][1])); mrg1<1>(st); }
  constexpr int e0 = 4 * a + 1 - jj, e1 = 4 * a + 3 - jj;
  if constexpr (e0 >= 0) st.acc[c][0] = mfma16<F16>(st.A[e0 >= 0 ? e0 : 0], st.B[F % 3], jj == 0 ? (f32x4){0.f, 0.f, 0.f, 0.f} : st.acc[c][0]);
  st.acc[c][1] = mfma16<F16>(st.A[e1], st.B[F % 3], jj == 0 ? (f32x4){0.f, 0.f, 0.f, 0.f} : st.acc[c][1]);
  if constexpr (a > 0 && rel == 1) mrg2(st);
  if constexpr (a > 0 && rel == 2) { mrg3<(a > 0 ? 2 * a - 1 : 0), F16>(st, yp, oscale); signal_nb(cy + 4 * (a - 1)); STAMPW(0, 2 + a); }
  if constexpr (rel == nrel - 6) mrg1<0>(st);
  if constexpr (rel == nrel - 4) mrg2(st);
  if constexpr (rel == nrel - 2) mrg3<2 * a, F16>(st, yp, oscale);
  __builtin_amdgcn_sched_barrier(0);
  if constexpr (F + 1 < 160) conv_step<F16, STREAMED, (F + 1 < 160 ? F + 1 : 0)>(st, xb, ab, yp, cx, cy, lane, oscale, ws);
}

__device__ __forceinline__ f32x2 cmul(f32x2 a, f32x2 b) {
  f32x2 t, r;
  asm("v_pk_mul_f32 %0, %1, %2 op_sel:[0,0] op_sel_hi:[0,1]" : "=v"(t) : "v"(a), "v"(b));
  asm("v_pk_fma_f32 %0, %1, %2, %3 op_sel:[1,1,0] op_sel_hi:[1,0,1] neg_lo:[1,0,0]" : "=v"(r) : "v"(a), "v"(b), "v"(t));
  return r;
}
__device__ __forceinline__ f32x2 cpow(float a, float b, float m) {
  float e = __builtin_amdgcn_exp2f((a * m) * 1.44269504088896341f);
  float th = b * m;
  const float I2PH = 0.15915494309189535f;
  const float I2PL = 6.4206383e-09f;
  float k = __builtin_rintf(th * I2PH);
  float f = __builtin_fmaf(th, I2PH, -k);
  f = __builtin_fmaf(th, I2PL, f);
  f32x2 z = {e * __builtin_amdgcn_cosf(f), e * __builtin_amdgcn_sinf(f)};
  return z;
}
__device__ __forceinline__ void pow2_scale(u32 mbits, int T, float& s, float& inv) {
  int e = (int)((mbits >> 23) & 0xff);
  e = e < 1 ? 1 : (e > 254 ? 254 : e);
  int se = 254 + T - e;
  se = se < 1 ? 1 : (se > 253 ? 253 : se);
  s = __uint_as_float((u32)se << 23);
  inv = __uint_as_float((u32)(254 - se) << 23);
}

struct Params {
  const u16 *q, *k, *v, *shk, *ssmk, *ssmD, *u, *Are, *Aim, *Bre, *Bim, *Cre, *Cim, *logdt, *Dskip;
  u16* out;
  char* ws;
};

template <bool F16, bool STREAMED>
__device__ __forceinline__ void conv_wave(lptr hs, lptr xs, lptr cx, lptr cy, int lane, float oscale, float dfold, char* ws) {
  Stream st;
  st.sv = 0.f;
  const int kg = lane >> 4, q = (lane & 15) >> 2, p = lane & 3;
  lptr ab = hs + (256 - 64 * kg - 8 * q + 8 * p);
  lptr xb = xs + (8 + 16 * kg + 8 * (lane & 15)) + ((lane & 1) ? 0 : XS2);
  lptr yp = hs + (kg >> 1) * 512 + (kg & 1) * 128 + 8 * (lane & 15);
  if (STREAMED) {
    if (lane == 0) *(LDS_AS u16*)(hs + 32) = (u16)pk_bf16(bf_lo(*(LDS_AS u16*)(hs + 32)) + dfold, 0.f);
    CFENCE();
  }
  load_afrag<1>(st, ab); load_afrag<3>(st, ab); load_afrag<0>(st, ab); load_afrag<2>(st, ab);
  if (STREAMED) wait_cnt(cx, 4);
  STAMPW(0, 2);
  st.B[0] = *(volatile LDS_AS s16x8*)(xb + frag_off(0));
  st.B[1] = *(volatile LDS_AS s16x8*)(xb + frag_off(1));
  conv_step<F16, STREAMED, 0>(st, xb, ab, yp, cx, cy, lane, oscale, ws);
  asm volatile("s_nop 7" : "+v"(st.acc[3][1]));
  mrg1<1>(st); mrg2(st); mrg3<7, F16>(st, yp, oscale);
  signal_nb(cy + 12);
  STAMPW(0, 6);
}

constexpr int RAW_U = 2112;
constexpr int A_RAW = 0;
constexpr int A_XS = A_RAW + 16 * RAW_U;
constexpr int A_HS = A_XS + XS_ALL;
constexpr int A_CNT = A_HS + 8 * HS_CH;
constexpr int A_END = A_CNT + 64;

template <int DC>
__device__ __forceinline__ void kv_block(lptr sm, lptr raw, int s, int lane, s16x8 W0, s16x8 W1) {
  const int r = lane & 15, kg = lane >> 4;
  lptr ap = raw + (8 * (r >> 2) + (r & 3) + 4 - kg) * 16;
  s16x8 a00 = *(LDS_AS s16x8*)(ap), a01 = *(LDS_AS s16x8*)(ap + 512);
  s16x8 a10 = *(LDS_AS s16x8*)(ap + 64), a11 = *(LDS_AS s16x8*)(ap + 64 + 512);
  const int q = (lane & 15) >> 2, p = lane & 3;
  lptr vp = raw + 1088 + (8 * (kg + 4 * (p >> 1)) + q) * 16 + 8 * (p & 1);
  u32x2 V0 = tr_read(vp), V1 = tr_read(vp + 64);
  f32x4 d0 = mfma16<false>(a00, W0, (f32x4){0.f, 0.f, 0.f, 0.f});
  f32x4 d1 = mfma16<false>(a10, W0, (f32x4){0.f, 0.f, 0.f, 0.f});
  d0 = mfma16<false>(a01, W1, d0);
  d1 = mfma16<false>(a11, W1, d1);
  const float v0[4] = {bf_lo(V0.x), bf_hi(V0.x), bf_lo(V0.y), bf_hi(V0.y)};
  const float v1[4] = {bf_lo(V1.x), bf_hi(V1.x), bf_lo(V1.y), bf_hi(V1.y)};
  const int ch = lane & 7, ip = kg + 4 * ((lane >> 3) & 1);
  lptr dst = sm + A_XS + ch * XS_CH + 128 + (128 * DC + 16 * s + 2 * ip) * 2;
#pragma unroll
  for (int c = 0; c < 4; ++c) {
    float a = d0[c], b = d1[c];
    rbf2(a, b);
    const u32 w = pk_bf16(fmul(a, v0[c]), fmul(b, v1[c]));
    *(LDS_AS u32*)(dst + c * XP) = w;
    *(LDS_AS u32*)(dst + c * XP + XS2) = w;
  }
}

template <int DT>
__device__ __forceinline__ void final_A(lptr sm, int s, int lane, u32x4 qv, u16* outrow0) {
  const int c = lane >> 4, q = (lane & 15) >> 2, p = lane & 3;
  const int yoff = q * HS_CH + 1024 * DT + (c >> 1) * 512 + (s >> 2) * 256 + (c & 1) * 128 + (s & 3) * 32 + 8 * p;
  u32x2 Y0 = tr_read(sm + A_HS + yoff), Y1 = tr_read(sm + A_HS + yoff + 4 * HS_CH);
  u32 yv[4] = {Y0.x, Y0.y, Y1.x, Y1.y};
  u32x4 o;
#pragma unroll
  for (int e = 0; e < 4; ++e) o[e] = pk_bf16(fmul(bf_lo(yv[e]), bf_lo(qv[e])), fmul(bf_hi(yv[e]), bf_hi(qv[e])));
  u16* dst = outrow0 + (size_t)(512 * DT) * 1280;
  asm volatile("global_store_dwordx4 %0, %1, off\n\ts_nop 1" :: "v"(dst), "v"(o) : "memory");
}

__device__ __forceinline__ void head_A(const Params& P, lptr sm, int ch0, char* ws) {
  const int tid = threadIdx.x, lane = tid & 63;
  const int wave = __builtin_amdgcn_readfirstlane(tid >> 6);
  const u32 lb = (u32)(size_t)sm;
  lptr cx = sm + A_CNT, cy = sm + A_CNT + 32;
  if (tid < 4) *(LDS_AS u32x4*)(sm + A_CNT + tid * 16) = (u32x4){0, 0, 0, 0};
  if (wave < 8) {
    u32 dv;
    asm volatile("global_load_ushort %0, %1, off" : "=v"(dv) : "v"(P.ssmD + ch0 + wave) : "memory");
#pragma unroll
    for (int i = 0; i < 4; ++i) {
      dma16((const char*)(P.ssmk + (size_t)(ch0 + wave) * S) + i * 1024 + lane * 16, lb + A_HS + wave * HS_CH + 32 + i * 1024);
      if (i == 0) BAR();
    }
    const u32x4 z = {0, 0, 0, 0};
    if (lane < 32) *(LDS_AS u32x4*)(sm + A_XS + wave * XS_CH + (lane >> 3) * XP + (lane & 7) * 16) = z;
    *(LDS_AS u32x2*)(sm + A_XS + XS2 + wave * XS_CH + (lane >> 4) * XP + (lane & 15) * 8) = (u32x2){0, 0};
    if (lane < 2) *(LDS_AS u32x4*)(sm + A_HS + wave * HS_CH + lane * 16) = z;
    WAITV(3);
    asm volatile("" : "+v"(dv));
    conv_wave<false, true>(sm + A_HS + wave * HS_CH, sm + A_XS + wave * XS_CH, cx, cy, lane, 1.0f, bf_lo(dv), ws);
  } else {
    __builtin_amdgcn_s_setprio(3);
    const int s = wave - 8;
    const char* kp = (const char*)(P.k + ch0) + (size_t)(s * 64 + lane) * 1280;
    const char* vp = (const char*)(P.v + ch0) + (size_t)(s * 64 + lane) * 1280;
    const u32 rawb = lb + A_RAW + s * 2 * RAW_U;
    lptr raw0 = sm + A_RAW + s * 2 * RAW_U, raw1 = raw0 + RAW_U;
#define ISSUE(d) do {                                                                                    \
      if ((d) > 0 || s > 0) { if (lane < 4) dma16(kp + ((size_t)(d) * 512 - 4) * 1280, rawb + ((d) & 1) * RAW_U); } \
      else if (lane < 4) *(LDS_AS u32x4*)(raw0 + lane * 16) = (u32x4){0, 0, 0, 0};                       \
      dma16(kp + (size_t)(d) * 512 * 1280, rawb + ((d) & 1) * RAW_U + 64);                               \
      dma16(vp + (size_t)(d) * 512 * 1280, rawb + ((d) & 1) * RAW_U + 1088);                             \
    } while (0)
    u32 wt;
    asm volatile("global_load_ushort %0, %1, off" : "=v"(wt) : "v"(P.shk + (ch0 + (lane & 7)) * 4 + (lane >> 4)) : "memory");
    ISSUE(0);
    BAR();
    ISSUE(1);
    const int frow = 4 * (16 * s + (lane & 15)) + (lane >> 4);
    const char* qp = (const char*)(P.q + ch0) + (size_t)frow * 1280;
    u16* op = P.out + (size_t)frow * 1280 + ch0;
    u32x4 qa, qb, qc, qd;
#define QLOAD(Q, dt) asm volatile("global_load_dwordx4 %0, %1, off nt" : "=v"(Q) : "v"(qp + (size_t)(dt) * 512 * 1280) : "memory")
    WAITV(3);
    asm volatile("" : "+v"(wt));
    STAMPW(15, 1);
    s16x8 W0, W1;
    {
      const int ch = lane & 7;
      const u32 wv = wt << (16 * (ch & 1));
      u32x4 w = {(ch >> 1) == 0 ? wv : 0u, (ch >> 1) == 1 ? wv : 0u, (ch >> 1) == 2 ? wv : 0u, (ch >> 1) == 3 ? wv : 0u};
      const u32x4 z = {0, 0, 0, 0};
      W0 = __builtin_bit_cast(s16x8, (lane & 8) ? z : w);
      W1 = __builtin_bit_cast(s16x8, (lane & 8) ? w : z);
    }
    lptr cxs = cx + (s >> 2) * 4;
    kv_block<0>(sm, raw0, s, lane, W0, W1);
    signal_nb(cxs + 0);
    asm volatile("s_waitcnt lgkmcnt(0)" ::: "memory");
    STAMPW(15, 2);
    wait_cnt(cx + 0, 4); wait_cnt(cx + 4, 4);
    ISSUE(2);
    WAITV(3);
    kv_block<1>(sm, raw1, s, lane, W0, W1);
    signal_nb(cxs + 8);
    asm volatile("s_waitcnt lgkmcnt(0)" ::: "memory");
    STAMPW(15, 3);
    wait_cnt(cx + 8, 4); wait_cnt(cx + 12, 4);
    QLOAD(qa, 0);
    ISSUE(3);
    WAITV(4);
    kv_block<2>(sm, raw0, s, lane, W0, W1);
    signal_nb(cxs + 16);
    wait_cnt(cx + 16, 4); wait_cnt(cx + 20, 4);
    QLOAD(qb, 1);
    wait_cnt(cy + 0, 8);
    WAITV(4);
    asm volatile("" : "+v"(qa));
    final_A<0>(sm, s, lane, qa, op);
    WAITV(2);
    kv_block<3>(sm, raw1, s, lane, W0, W1);
    signal_nb(cxs + 24);
    STAMPW(15, 4);
    wait_cnt(cx + 24, 4); wait_cnt(cx + 28, 4);
    QLOAD(qc, 2);
    QLOAD(qd, 3);
    wait_cnt(cy + 4, 8);
    WAITV(3);
    asm volatile("" : "+v"(qb));
    final_A<1>(sm, s, lane, qb, op);
    wait_cnt(cy + 8, 8);
    WAITV(1);
    asm volatile("" : "+v"(qc), "+v"(qd));
    final_A<2>(sm, s, lane, qc, op);
    STAMPW(15, 5);
    wait_cnt(cy + 12, 8);
    final_A<3>(sm, s, lane, qd, op);
#undef ISSUE
#undef QLOAD
  }
}

constexpr int B_RAW = 0;
constexpr int B_XS = 32768;
constexpr int B_TB = B_XS;
constexpr int B_TST = 4352;
constexpr int B_HS = B_XS + XS_ALL;
constexpr int B_KP = B_HS + 8 * HS_CH;
constexpr int B_CNT = B_KP + 8 * 1312;
constexpr int B_END = B_CNT + 128;

template <int DT>
__device__ __forceinline__ void final_B(lptr sm, int s, int lane, u16* outrow0) {
  const int c = lane >> 4, q = (lane & 15) >> 2, p = lane & 3;
  const int yoff = q * HS_CH + 1024 * DT + (c >> 1) * 512 + (s >> 2) * 256 + (c & 1) * 128 + (s & 3) * 32 + 8 * p;
  u32x2 Y0 = tr_read(sm + B_HS + yoff), Y1 = tr_read(sm + B_HS + yoff + 4 * HS_CH);
  u32x4 o = {Y0.x, Y0.y, Y1.x, Y1.y};
  u16* dst = outrow0 + (size_t)(512 * DT) * 1280;
  asm volatile("global_store_dwordx4 %0, %1, off\n\ts_nop 1" :: "v"(dst), "v"(o) : "memory");
}

__device__ __forceinline__ void head_B(const Params& P, lptr sm, int ch0, int g, char* ws) {
  const int tid = threadIdx.x, lane = tid & 63;
  const int wave = __builtin_amdgcn_readfirstlane(tid >> 6);
  const int w8 = wave & 7, half = wave >> 3;
  const u32 lb = (u32)(size_t)sm;
  const int d = ch0 + w8;
  u32 pr[6];
  u32 cw;
  u32 ldt;
  {
    const size_t po = (size_t)d * NM * 2 + lane * 4;
    asm volatile("global_load_dword %0, %1, off" : "=v"(pr[0]) : "v"((const char*)P.Are + po) : "memory");
    asm volatile("global_load_dword %0, %1, off" : "=v"(pr[1]) : "v"((const char*)P.Aim + po) : "memory");
    asm volatile("global_load_dword %0, %1, off" : "=v"(pr[2]) : "v"((const char*)P.Bre + po) : "memory");
    asm volatile("global_load_dword %0, %1, off" : "=v"(pr[3]) : "v"((const char*)P.Bim + po) : "memory");
    asm volatile("global_load_dword %0, %1, off" : "=v"(pr[4]) : "v"((const char*)P.Cre + po) : "memory");
    asm volatile("global_load_dword %0, %1, off" : "=v"(pr[5]) : "v"((const char*)P.Cim + po) : "memory");
    asm volatile("global_load_ushort %0, %1, off" : "=v"(ldt) : "v"(P.logdt + d) : "memory");
    asm volatile("global_load_ushort %0, %1, off" : "=v"(cw) : "v"(P.Dskip + d) : "memory");
  }
#pragma unroll
  for (int c = 0; c < 2; ++c) {
    const int rb = (g * 256 + c * 1024 + wave * 64) & 2047;
    dma16((const char*)(P.u + ch0) + (size_t)(rb + lane) * 1280, lb + B_RAW + rb * 16);
  }
  {
    const u32x4 z = {0, 0, 0, 0};
    if (tid < 8) *(LDS_AS u32x4*)(sm + B_CNT + tid * 16) = z;
  }
  WAITV(2);
  asm volatile("" : "+v"(pr[0]), "+v"(pr[1]), "+v"(pr[2]), "+v"(pr[3]), "+v"(pr[4]), "+v"(pr[5]), "+v"(ldt), "+v"(cw));
  STAMPW(0, 1);
  lptr tb = sm + B_TB + wave * B_TST;
  constexpr int RS = 272;
  const float dt = rbf(expf(bf_lo(ldt)));
  float cs, cinv;
  f32x2 G[8];
  float aa, bb;
  {
    f32x2 c0 = cmul((f32x2){bf_lo(pr[4]), bf_lo(pr[5])}, (f32x2){dt * bf_lo(pr[2]), dt * bf_lo(pr[3])});
    f32x2 c1 = cmul((f32x2){bf_hi(pr[4]), bf_hi(pr[5])}, (f32x2){dt * bf_hi(pr[2]), dt * bf_hi(pr[3])});
    float sb = fabsf(c0.x) + fabsf(c0.y) + fabsf(c1.x) + fabsf(c1.y);
#pragma unroll
    for (int o = 32; o >= 1; o >>= 1) sb += __shfl_xor(sb, o);
    sb += fabsf(bf_lo(cw));
    pow2_scale(__float_as_uint(sb), 14, cs, cinv);
    G[0] = half ? c1 : c0;
    G[0] *= cs;
    aa = dt * (half ? bf_hi(pr[0]) : bf_lo(pr[0]));
    bb = dt * (half ? bf_hi(pr[1]) : bf_lo(pr[1]));
  }
  lptr tw = tb + lane * 4;
  lptr trd = tb + (lane & 15) * RS + (lane >> 4) * 16;
  {
    f32x2 z8 = cpow(aa, -bb, 8.f);
    f32x2 f = z8;
    *(LDS_AS u32*)(tw) = pk_f16(1.f, 0.f);
#pragma unroll
    for (int j = 1; j < 16; ++j) {
      *(LDS_AS u32*)(tw + j * RS) = pk_f16v(f);
      if (j < 15) f = cmul(f, z8);
    }
  }
  CFENCE();
  f16x8 Ff[4];
#pragma unroll
  for (int ks = 0; ks < 4; ++ks) Ff[ks] = *(LDS_AS f16x8*)(trd + ks * 64);
  CFENCE();
  f32x4 kacc[3];
  const f32x2 v1 = cpow(aa, bb, 128.f);
  {
    const f32x2 z1 = cpow(aa, bb, 1.f);
#pragma unroll
    for (int p = 1; p < 8; ++p) G[p] = cmul(G[p - 1], z1);
#pragma unroll
    for (int p = 0; p < 8; ++p) {
      *(LDS_AS u32*)(tw + p * RS) = pk_f16v(G[p]);
      *(LDS_AS u32*)(tw + (8 + p) * RS) = pk_f16v(cmul(G[p], v1));
    }
    CFENCE();
    kacc[0] = (f32x4){0.f, 0.f, 0.f, 0.f};
#pragma unroll
    for (int ks = 0; ks < 4; ++ks)
      kacc[0] = __builtin_amdgcn_mfma_f32_16x16x32_f16(*(LDS_AS f16x8*)(trd + ks * 64), Ff[ks], kacc[0], 0, 0, 0);
    CFENCE();
  }
  {
    const f32x2 v2 = cpow(aa, bb, 256.f), v4 = cpow(aa, bb, 512.f);
#pragma unroll
    for (int p = 0; p < 4; ++p) {
      f32x2 t = cmul(G[2 * p], v2);
      *(LDS_AS u32*)(tw + p * RS) = pk_f16v(t);
      *(LDS_AS u32*)(tw + (4 + p) * RS) = pk_f16v(cmul(t, v1));
    }
#pragma unroll
    for (int p = 0; p < 2; ++p) {
      f32x2 t = cmul(G[4 * p], v4);
#pragma unroll
      for (int a = 0; a < 4; ++a) {
        *(LDS_AS u32*)(tw + (8 + 2 * a + p) * RS) = pk_f16v(t);
        if (a < 3) t = cmul(t, v1);
      }
    }
    CFENCE();
    kacc[1] = (f32x4){0.f, 0.f, 0.f, 0.f};
#pragma unroll
    for (int ks = 0; ks < 4; ++ks)
      kacc[1] = __builtin_amdgcn_mfma_f32_16x16x32_f16(*(LDS_AS f16x8*)(trd + ks * 64), Ff[ks], kacc[1], 0, 0, 0);
    CFENCE();
  }
  {
    f32x2 t = cmul(G[0], cpow(aa, bb, 1024.f));
#pragma unroll
    for (int a = 0; a < 9; ++a) {
      *(LDS_AS u32*)(tw + a * RS) = pk_f16v(t);
      if (a < 8) t = cmul(t, v1);
    }
    CFENCE();
    kacc[2] = (f32x4){0.f, 0.f, 0.f, 0.f};
#pragma unroll
    for (int ks = 0; ks < 4; ++ks)
      kacc[2] = __builtin_amdgcn_mfma_f32_16x16x32_f16(*(LDS_AS f16x8*)(trd + ks * 64), Ff[ks], kacc[2], 0, 0, 0);
    CFENCE();
  }
  if (half) {
#pragma unroll
    for (int t = 0; t < 3; ++t) *(LDS_AS f32x4*)(tb + t * 1024 + lane * 16) = kacc[t];
  }
  STAMPW(0, 2);
  WAITV(0);
  BAR();
  STAMPW(0, 3);
  lptr cy = sm + B_CNT + 32;
  if (!half) {
    lptr ob = sm + B_TB + (wave + 8) * B_TST;
    const int r16 = lane & 15, kg = lane >> 4;
    lptr Q = sm + B_KP + wave * 1312;
#pragma unroll
    for (int t = 0; t < 3; ++t) {
      f32x4 o = *(LDS_AS f32x4*)(ob + t * 1024 + lane * 16);
      o += kacc[t];
      if (t == 0 && lane == 0) o[0] += bf_lo(cw) * cs;
      u32 a = pk_f16(o[0], o[1]), b = pk_f16(o[2], o[3]);
      if (t == 0) {
        u32x2 w = {a, b};
        *(LDS_AS u32x2*)(Q + 2 * (4 * (kg & 1) + 128 * (kg >> 1) + 8 * r16)) = w;
      } else if (t == 1) {
        if (kg < 2) {
          u32x2 w = {a, b};
          *(LDS_AS u32x2*)(Q + 2 * (256 + 64 * kg + 4 * r16)) = w;
        } else {
          *(LDS_AS u32*)(Q + 2 * (384 + 64 * (kg - 2) + 2 * r16)) = a;
          *(LDS_AS u32*)(Q + 2 * (384 + 64 * (kg - 2) + 32 + 2 * r16)) = b;
        }
      } else {
        if (kg < 2) {
          *(LDS_AS u16*)(Q + 2 * (512 + 64 * kg + r16)) = (u16)a;
          *(LDS_AS u16*)(Q + 2 * (512 + 64 * kg + 16 + r16)) = (u16)(a >> 16);
          *(LDS_AS u16*)(Q + 2 * (512 + 64 * kg + 32 + r16)) = (u16)b;
          *(LDS_AS u16*)(Q + 2 * (512 + 64 * kg + 48 + r16)) = (u16)(b >> 16);
        } else if (lane == 32) {
          *(LDS_AS u16*)(Q + 2 * 640) = (u16)a;
        }
      }
    }
  }
  u32x2 U[8];
  if (half) {
    const int gq = lane >> 4, q = (lane & 15) >> 2, p = lane & 3;
    const int off = (256 * w8 + 8 * (2 * gq + (p >> 1)) + q) * 16 + 8 * (p & 1);
#pragma unroll
    for (int s = 0; s < 4; ++s) {
      U[2 * s] = tr_read(sm + B_RAW + off + s * 1024);
      U[2 * s + 1] = tr_read(sm + B_RAW + off + s * 1024 + 64);
    }
    u16x2 mx = {0, 0};
#pragma unroll
    for (int s = 0; s < 8; ++s) {
      mx = __builtin_elementwise_max(mx, __builtin_bit_cast(u16x2, U[s].x & 0x7fff7fffu));
      mx = __builtin_elementwise_max(mx, __builtin_bit_cast(u16x2, U[s].y & 0x7fff7fffu));
    }
    u32 m1 = max((u32)mx.x, (u32)mx.y);
    m1 = max(m1, (u32)__shfl_xor((int)m1, 8));
    m1 = max(m1, (u32)__shfl_xor((int)m1, 16));
    m1 = max(m1, (u32)__shfl_xor((int)m1, 32));
    if (lane < 8) __hip_atomic_fetch_max((LDS_AS u32*)(sm + B_CNT + lane * 4), m1, __ATOMIC_RELAXED, __HIP_MEMORY_SCOPE_WORKGROUP);
  }
  BAR();
  if (!half) {
    lptr Q = sm + B_KP + wave * 1312;
    lptr hs = sm + B_HS + wave * HS_CH;
    if (lane < 2) *(LDS_AS u32x4*)(hs + lane * 16) = (u32x4){0, 0, 0, 0};
    if (lane < 32) *(LDS_AS u32x4*)(sm + B_XS + wave * XS_CH + (lane >> 3) * XP + (lane & 7) * 16) = (u32x4){0, 0, 0, 0};
    *(LDS_AS u32x2*)(sm + B_XS + XS2 + wave * XS_CH + (lane >> 4) * XP + (lane & 15) * 8) = (u32x2){0, 0};
    if (lane < 32) {
      *(LDS_AS u32x4*)(hs + 32 + lane * 16) = *(LDS_AS u32x4*)(Q + lane * 16);
    } else {
      const int i = lane - 32;
      u32x2 w = *(LDS_AS u32x2*)(Q + 2 * (256 + 4 * i));
      u32 v4 = *(LDS_AS u16*)(Q + 2 * (256 + 4 * i + 4));
      u32x4 o;
      o.x = __builtin_amdgcn_perm(w.x, w.x, 0x01000100);
      o.y = __builtin_amdgcn_perm(w.y, w.x, 0x05040302);
      o.z = __builtin_amdgcn_perm(w.y, w.y, 0x01000100);
      o.w = __builtin_amdgcn_perm(v4, w.y, 0x05040302);
      *(LDS_AS u32x4*)(hs + 32 + 512 + i * 16) = o;
    }
    {
      u32 w = *(LDS_AS u32*)(Q + 2 * (384 + 2 * lane));
      u32 v2 = *(LDS_AS u16*)(Q + 2 * (384 + 2 * lane + 2));
      u32x4 o;
      o.x = __builtin_amdgcn_perm(w, w, 0x01000100);
      o.y = w;
      o.z = __builtin_amdgcn_perm(w, w, 0x03020302);
      o.w = __builtin_amdgcn_perm(v2, v2, 0x01000100);
      *(LDS_AS u32x4*)(hs + 32 + 1024 + lane * 16) = o;
    }
    {
      u32 w = *(LDS_AS u32*)(Q + 2 * (512 + 2 * lane));
      u32 v2 = *(LDS_AS u16*)(Q + 2 * (512 + 2 * lane + 2));
      u32 a0 = __builtin_amdgcn_perm(w, w, 0x01000100), a1 = __builtin_amdgcn_perm(w, w, 0x03020302), a2 = __builtin_amdgcn_perm(v2, v2, 0x01000100);
      u32x4 o0 = {a0, a0, w, a1}, o1 = {a1, a1, a2, a2};
      *(LDS_AS u32x4*)(hs + 32 + 2048 + lane * 32) = o0;
      *(LDS_AS u32x4*)(hs + 32 + 2048 + lane * 32 + 16) = o1;
    }
  } else {
    const int ch = lane & 7, ip = lane >> 3;
    float us, uinv;
    pow2_scale(*(LDS_AS u32*)(sm + B_CNT + ch * 4) << 16, 12, us, uinv);
    lptr dst = sm + B_XS + ch * XS_CH + 128 + (64 * w8 + 2 * ip) * 2;
#pragma unroll
    for (int s = 0; s < 4; ++s) {
      u32x2 a = U[2 * s], b = U[2 * s + 1];
      u32 w[4] = {pk_f16(bf_lo(a.x) * us, bf_lo(b.x) * us), pk_f16(bf_hi(a.x) * us, bf_hi(b.x) * us),
                  pk_f16(bf_lo(a.y) * us, bf_lo(b.y) * us), pk_f16(bf_hi(a.y) * us, bf_hi(b.y) * us)};
#pragma unroll
      for (int c = 0; c < 4; ++c) {
        *(LDS_AS u32*)(dst + s * 32 + c * XP) = w[c];
        *(LDS_AS u32*)(dst + s * 32 + c * XP + XS2) = w[c];
      }
    }
  }
  BAR();
  STAMPW(15, 1);
  if (!half) {
    float us2, uinv2;
    pow2_scale(*(LDS_AS u32*)(sm + B_CNT + wave * 4) << 16, 12, us2, uinv2);
    conv_wave<true, false>(sm + B_HS + wave * HS_CH, sm + B_XS + wave * XS_CH, cy, cy, lane, uinv2 * cinv, 0.f, ws + 8 * 8 * 256);
  } else {
    const int frow = 4 * (16 * w8 + (lane & 15)) + (lane >> 4);
    u16* op = P.out + (size_t)frow * 1280 + 640 + ch0;
    wait_cnt(cy + 0, 8);
    final_B<0>(sm, w8, lane, op);
    wait_cnt(cy + 4, 8);
    final_B<1>(sm, w8, lane, op);
    wait_cnt(cy + 8, 8);
    final_B<2>(sm, w8, lane, op);
    wait_cnt(cy + 12, 8);
    final_B<3>(sm, w8, lane, op);
  }
}

__global__ __launch_bounds__(1024) void k_fused(Params P) {
  __shared__ __attribute__((aligned(256))) char smem[A_END > B_END ? A_END : B_END];
  lptr sm = (lptr)smem;
  char* ws = P.ws;
  STAMPW(0, 0);
  STAMPW(15, 0);
  const int b = blockIdx.x;
  const int xcd = b & 7, slot = b >> 3;
  int s0, s1, s2 = -1;
  if (xcd < 2) { s0 = 2 * xcd; s1 = 2 * xcd + 1; }
  else if (xcd < 6) { s0 = 2 + xcd; s1 = 10 + 2 * (xcd - 2); s2 = s1 + 1; }
  else { s0 = 2 + xcd; s1 = 12 + xcd; }
  const int ns = s2 >= 0 ? 3 : 2;
  if (slot < ns * 8) {
    const int si = slot >> 3, g = slot & 7;
    const int s = si == 0 ? s0 : (si == 1 ? s1 : s2);
    if (s < 10) head_A(P, sm, s * 64 + g * 8, ws);
    else head_B(P, sm, (s - 10) * 64 + g * 8, g, ws);
  } else {
    const int hidx = slot - ns * 8, hn = 32 - ns * 8;
    const int hsn = xcd < 2 ? 2 : 1;
    const int per = hn / hsn;
    const int sl = s0 + hidx / per, hi = hidx % per;
    const int tid = threadIdx.x;
    const int i = (tid >> 6) * (per * 64) + hi * 64 + (tid & 63);
    if (i < 5120) {
      const char* p;
      const int blk = i >> 9, r = i & 511;
      const int dc = blk < 9 ? blk / 3 : 3, ty = blk < 9 ? blk % 3 : 2;
      if (ty == 2) p = (const char*)P.q + (size_t)(dc * 512 + r) * 1280 + sl * 128;
      else p = (const char*)(ty ? P.v : P.k) + (size_t)((dc + 1) * 512 + r) * 1280 + sl * 128;
      u32 rr;
      asm volatile("global_load_dword %0, %1, off nt\n\ts_waitcnt vmcnt(0)" : "=v"(rr) : "v"(p) : "memory");
    }
  }
}

extern "C" void kernel_launch(void* const* d_in, const int* in_sizes, int n_in,
                              void* d_out, int out_size, void* d_ws, size_t ws_size,
                              hipStream_t stream) {
  Params P;
  P.q = (const u16*)d_in[0]; P.k = (const u16*)d_in[1]; P.v = (const u16*)d_in[2];
  P.shk = (const u16*)d_in[3]; P.ssmk = (const u16*)d_in[4]; P.ssmD = (const u16*)d_in[5];
  P.u = (const u16*)d_in[6]; P.Are = (const u16*)d_in[7]; P.Aim = (const u16*)d_in[8];
  P.Bre = (const u16*)d_in[9]; P.Bim = (const u16*)d_in[10]; P.Cre = (const u16*)d_in[11];
  P.Cim = (const u16*)d_in[12]; P.logdt = (const u16*)d_in[13]; P.Dskip = (const u16*)d_in[14];
  P.out = (u16*)d_out; P.ws = (char*)d_ws;
  k_fused<<<256, 1024, 0, stream>>>(P);
}
```

```cpp
#include <hip/hip_runtime.h>
#include <hip/hip_bf16.h>

typedef unsigned int u32;
typedef unsigned short u16;
typedef __attribute__((ext_vector_type(2))) u32 u32x2;
typedef __attribute__((ext_vector_type(4))) u32 u32x4;
typedef __attribute__((ext_vector_type(4))) short s16x4;
typedef __attribute__((ext_vector_type(8))) short s16x8;
typedef __attribute__((ext_vector_type(8))) __bf16 bf16x8;
typedef __attribute__((ext_vector_type(8))) _Float16 f16x8;
typedef __attribute__((ext_vector_type(2))) __bf16 bf16x2;
typedef __attribute__((ext_vector_type(2))) _Float16 f16x2;
typedef __attribute__((ext_vector_type(2))) unsigned short u16x2;
typedef __attribute__((ext_vector_type(2))) float f32x2;
typedef __attribute__((ext_vector_type(4))) float f32x4;
#define LDS_AS __attribute__((address_space(3)))
typedef LDS_AS char* lptr;


#define PDEPTH 2
#define NPF 2
#define UPOS 4
#define UEARLY 3
#define EARLYW 4
#define UTGT 5

constexpr int S = 2048;
constexpr int D = 640;
constexpr int NM = 128;

__device__ __forceinline__ float bf_lo(u32 v) { return __uint_as_float(v << 16); }
__device__ __forceinline__ float bf_hi(u32 v) { return __uint_as_float(v & 0xffff0000u); }
__device__ __forceinline__ u32 pk_bf16(float a, float b) {
  f32x2 v = {a, b};
  return __builtin_bit_cast(u32, __builtin_convertvector(v, bf16x2));
}
__device__ __forceinline__ u32 pk_f16(float a, float b) {
  f32x2 v = {a, b};
  return __builtin_bit_cast(u32, __builtin_convertvector(v, f16x2));
}
__device__ __forceinline__ u32 pk_f16v(f32x2 v) { return __builtin_bit_cast(u32, __builtin_convertvector(v, f16x2)); }
__device__ __forceinline__ float rbf(float a) { return bf_lo(pk_bf16(a, a)); }
__device__ __forceinline__ void rbf2(float& a, float& b) { u32 p = pk_bf16(a, b); a = bf_lo(p); b = bf_hi(p); }

#define WAITV(n) asm volatile("s_waitcnt vmcnt(" #n ")" ::: "memory")
#define BAR() do { asm volatile("s_waitcnt lgkmcnt(0)" ::: "memory"); __builtin_amdgcn_s_barrier(); asm volatile("" ::: "memory"); } while (0)
#define CFENCE() asm volatile("" ::: "memory")

__device__ __forceinline__ void dma16(const void* g, u32 lds) {
  u32 keep;
  lds = __builtin_amdgcn_readfirstlane(lds);
  asm volatile("s_mov_b32 %0, m0\n\ts_mov_b32 m0, %2\n\ts_nop 0\n\tglobal_load_lds_dwordx4 %1, off\n\ts_mov_b32 m0, %0"
               : "=&s"(keep) : "v"(g), "s"(lds) : "memory");
}
__device__ __forceinline__ void dma16h(const void* g, u32 lds) {
  u32 keep;
  lds = __builtin_amdgcn_readfirstlane(lds);
  asm volatile("s_mov_b32 %0, m0\n\ts_mov_b32 m0, %2\n\ts_nop 0\n\tglobal_load_lds_dwordx4 %1, off\n\ts_mov_b32 m0, %0"
               : "=&s"(keep) : "v"(g), "s"(lds) : "memory");
}
__device__ __forceinline__ void dma4(const void* g, u32 lds) {
  u32 keep;
  lds = __builtin_amdgcn_readfirstlane(lds);
  asm volatile("s_mov_b32 %0, m0\n\ts_mov_b32 m0, %2\n\ts_nop 0\n\tglobal_load_lds_dword %1, off\n\ts_mov_b32 m0, %0"
               : "=&s"(keep) : "v"(g), "s"(lds) : "memory");
}
__device__ __forceinline__ u32x2 tr_read(lptr p) {
  return __builtin_bit_cast(u32x2, __builtin_amdgcn_ds_read_tr16_b64_v4i16((LDS_AS s16x4*)p));
}
__device__ __forceinline__ void wait_cnt(lptr p, u32 target) {
  CFENCE();
  while ((u32)__builtin_amdgcn_readfirstlane(*(volatile LDS_AS u32*)p) < target) __builtin_amdgcn_s_sleep(1);
  CFENCE();
}
__device__ __forceinline__ void wait_cnt_slow(lptr p, u32 target, bool urgent) {
  CFENCE();
  while ((u32)__builtin_amdgcn_readfirstlane(*(volatile LDS_AS u32*)p) < target) {
    if (urgent) __builtin_amdgcn_s_sleep(1); else __builtin_amdgcn_s_sleep(8);
  }
  CFENCE();
}
__device__ __forceinline__ void signal(lptr p, int lane) {
  CFENCE();
  if (lane == 0) __hip_atomic_fetch_add((LDS_AS u32*)p, 1u, __ATOMIC_RELAXED, __HIP_MEMORY_SCOPE_WORKGROUP);
  CFENCE();
}

__device__ __forceinline__ void signal_nb(lptr p) {
  asm volatile("s_mov_b64 exec, 1\n\tds_add_u32 %0, %1\n\ts_mov_b64 exec, -1" :: "v"((u32)(size_t)p), "v"(1u) : "memory");
}

constexpr int XP = 1152;
constexpr int XS_CH = 4640;
constexpr int XS2 = 8 * XS_CH + 128 + 8;
constexpr int XS_ALL = 2 * 8 * XS_CH + 256;
constexpr int HS_CH = 4128;

template <bool F16>
__device__ __forceinline__ f32x4 mfma16(s16x8 a, s16x8 b, f32x4 c) {
  if (F16) return __builtin_amdgcn_mfma_f32_16x16x32_f16(__builtin_bit_cast(f16x8, a), __builtin_bit_cast(f16x8, b), c, 0, 0, 0);
  else return __builtin_amdgcn_mfma_f32_16x16x32_bf16(__builtin_bit_cast(bf16x8, a), __builtin_bit_cast(bf16x8, b), c, 0, 0, 0);
}

__device__ __forceinline__ float fadd(float a, float b) { float r; asm("v_add_f32 %0, %1, %2" : "=v"(r) : "v"(a), "v"(b)); return r; }
__device__ __forceinline__ float fmul(float a, float b) { float r; asm("v_mul_f32 %0, %1, %2" : "=v"(r) : "v"(a), "v"(b)); return r; }
__device__ __forceinline__ void sw16(float& a, float& b) {
  auto r = __builtin_amdgcn_permlane16_swap(__float_as_uint(a), __float_as_uint(b), false, false);
  a = __uint_as_float(r[0]); b = __uint_as_float(r[1]);
}
__device__ __forceinline__ void sw32(float& a, float& b) {
  auto r = __builtin_amdgcn_permlane32_swap(__float_as_uint(a), __float_as_uint(b), false, false);
  a = __uint_as_float(r[0]); b = __uint_as_float(r[1]);
}
__device__ __forceinline__ void tr4(float& a, float& b, float& c, float& d) { sw32(a, c); sw32(b, d); sw16(a, b); sw16(c, d); }

template <bool F16> __device__ __forceinline__ void mfma_acc(f32x4& d, s16x8 a, s16x8 b) {
  if (F16) asm volatile("v_mfma_f32_16x16x32_f16 %0, %1, %2, %0" : "+v"(d) : "v"(a), "v"(b));
  else asm volatile("v_mfma_f32_16x16x32_bf16 %0, %1, %2, %0" : "+v"(d) : "v"(a), "v"(b));
}
template <bool F16> __device__ __forceinline__ void mfma_first(f32x4& d, s16x8 a, s16x8 b) {
  if (F16) asm volatile("v_mfma_f32_16x16x32_f16 %0, %1, %2, 0" : "=&v"(d) : "v"(a), "v"(b));
  else asm volatile("v_mfma_f32_16x16x32_bf16 %0, %1, %2, 0" : "=&v"(d) : "v"(a), "v"(b));
}
struct Stream {
  s16x8 A[16];
  f32x4 acc[4][8];
  s16x8 B[PDEPTH + 1];
  float c0, c1, c2, c3, l0, l1, l2, l3, sv;
  u32 cnt;
  int rowoff;
};
struct Seg { int t0, nt, j0, nj; };
constexpr int nseg(int SC) { return SC == 0 ? 6 : 1; }
constexpr Seg seg(int SC, int i) {
  if (SC == 1) return Seg{0, 4, 0, 8};
  if (SC == 2) return Seg{4, 2, 0, 12};
  if (SC == 3) return Seg{6, 2, 0, 16};
  switch (i) {
    case 0: return Seg{0, 4, 0, 4};
    case 1: return Seg{4, 2, 0, 4};
    case 2: return Seg{2, 4, 4, 4};
    case 3: return Seg{6, 2, 0, 8};
    case 4: return Seg{4, 4, 8, 4};
    default: return Seg{6, 2, 12, 4};
  }
}
constexpr int seg_base(int SC, int i) { int b = 0; for (int k = 0; k < i; ++k) b += 4 * seg(SC, k).nj; return b; }
constexpr int nstep(int SC) { return seg_base(SC, nseg(SC)); }
constexpr int seg_of(int SC, int F) { int k = 0; while (k + 1 < nseg(SC) && F >= seg_base(SC, k + 1)) ++k; return k; }
constexpr int step_jj(int SC, int F) { return seg(SC, seg_of(SC, F)).j0 + ((F - seg_base(SC, seg_of(SC, F))) >> 2); }
constexpr int frag_off(int SC, int F) { return (F & 3) * XP + 64 * step_jj(SC, F); }
constexpr int tile_ending_at(int SC, int F) {
  if (F < 0 || (F & 3) != 3) return -1;
  const int jj = step_jj(SC, F);
  const Seg g = seg(SC, seg_of(SC, F));
  if ((jj & 1) == 0) return -1;
  const int T = (jj - 1) / 2;
  return (T >= g.t0 && T < g.t0 + g.nt) ? T : -1;
}
constexpr int block_first_used_at(int SC, int F) {
  if (F >= nstep(SC) || (F & 3) != 0) return -1;
  const int jj = step_jj(SC, F);
  for (int f = 0; f < F; f += 4) if (step_jj(SC, f) == jj) return -1;
  return jj;
}
constexpr bool uses_e(int SC, int F, int e) {
  const Seg g = seg(SC, seg_of(SC, F));
  const int d = e - 1 + step_jj(SC, F);
  return d >= 0 && (d & 1) == 0 && d / 2 >= g.t0 && d / 2 < g.t0 + g.nt;
}
constexpr bool needs_load(int SC, int F, int e) {
  if (F >= nstep(SC) || !uses_e(SC, F, e)) return false;
  for (int k = 1; k <= 3; ++k) if (F - 4 * k >= 0 && uses_e(SC, F - 4 * k, e)) return false;
  return true;
}
template <int T> __device__ __forceinline__ void mrg1(Stream& st) {
  st.c0 = fadd(fadd(st.acc[1][T][3], st.acc[2][T][2]), st.acc[3][T][1]);
  st.c1 = fadd(st.acc[2][T][3], st.acc[3][T][2]);
  st.c2 = st.acc[3][T][3];
  st.l0 = st.acc[0][T][0];
  st.l1 = fadd(st.acc[0][T][1], st.acc[1][T][0]);
  st.l2 = fadd(fadd(st.acc[0][T][2], st.acc[1][T][1]), st.acc[2][T][0]);
  st.l3 = fadd(fadd(st.acc[0][T][3], st.acc[1][T][2]), fadd(st.acc[2][T][1], st.acc[3][T][0]));
}
__device__ __forceinline__ void mrg2(Stream& st) {
  st.c3 = 0.f;
  tr4(st.l0, st.l1, st.l2, st.l3);
  tr4(st.c0, st.c1, st.c2, st.c3);
}
template <int YOFF, bool SC> __device__ __forceinline__ void mrg3(Stream& st, lptr yp, float oscale) {
  float prev = __uint_as_float(__builtin_amdgcn_update_dpp(__float_as_uint(st.sv), __float_as_uint(st.c3), 0x111, 0xf, 0xf, false));
  st.sv = __uint_as_float(__builtin_amdgcn_update_dpp(0u, __float_as_uint(st.c3), 0x121, 0xf, 0xf, false));
  float y0 = fadd(st.l0, prev), y1 = fadd(st.l1, st.c0), y2 = fadd(st.l2, st.c1), y3 = fadd(st.l3, st.c2);
  if (SC) { y0 = fmul(y0, oscale); y1 = fmul(y1, oscale); y2 = fmul(y2, oscale); y3 = fmul(y3, oscale); }
  u32x2 w = {pk_bf16(y0, y1), pk_bf16(y2, y3)};
  *(LDS_AS u32x2*)(yp + YOFF) = w;
}
template <int E> __device__ __forceinline__ void load_afrag(Stream& st, lptr ab) {
  s16x4 lo = __builtin_amdgcn_ds_read_tr16_b64_v4i16((LDS_AS s16x4*)(ab + 256 * E));
  s16x4 hi = __builtin_amdgcn_ds_read_tr16_b64_v4i16((LDS_AS s16x4*)(ab + 256 * E - 32));
  st.A[E] = __builtin_shufflevector(lo, hi, 0, 1, 2, 3, 4, 5, 6, 7);
}
template <int SC, int G, int K, int E> __device__ __forceinline__ void load_needed(Stream& st, lptr ab) {
  if constexpr (E < 16) {
    if constexpr (needs_load(SC, G, E)) {
      if constexpr (K == 0) load_afrag<E>(st, ab);
      else load_needed<SC, G, K - 1, E + 1>(st, ab);
    } else load_needed<SC, G, K, E + 1>(st, ab);
  }
}
template <int SC, int F>
__device__ __forceinline__ void conv_step(Stream& st, lptr xb, lptr ab, lptr yp, lptr cx, lptr cy, float oscale) {
  constexpr int PD = PDEPTH;
  constexpr bool STREAMED = SC == 0, F16 = SC != 0;
  constexpr Seg g = seg(SC, seg_of(SC, F));
  constexpr int jj = step_jj(SC, F), c = F & 3;
  {
    constexpr int bE = block_first_used_at(SC, F + PD + 2), bN = block_first_used_at(SC, F + PD);
    constexpr u32 TG = STREAMED ? 2 : 1;
    lptr bx = STREAMED ? cx : cy + 320;
    if constexpr (bE > 0) st.cnt = *(volatile LDS_AS u32*)(bx + 4 * (bE > 0 ? bE : 0));
    if constexpr (bN > 0) {
      if ((u32)__builtin_amdgcn_readfirstlane(st.cnt) < TG) wait_cnt(bx + 4 * (bN > 0 ? bN : 0), TG);
      CFENCE();
    }
  }
  if constexpr (STREAMED) {
    if constexpr (F == 12) WAITV(1);
    if constexpr (F == 44) WAITV(0);
    if constexpr (F == 52) __builtin_amdgcn_s_setprio(1);
    if constexpr (F == 84) __builtin_amdgcn_s_setprio(0);
  }
  if constexpr (F + PD < nstep(SC)) st.B[(F + PD) % (PD + 1)] = *(volatile LDS_AS s16x8*)(xb + frag_off(SC, F + PD));
  load_needed<SC, (F & ~3) + 4, c, 0>(st, ab);
#pragma unroll
  for (int t = 0; t < g.nt; ++t) {
    const int T = g.t0 + t, e = 2 * T + 1 - jj;
    if (e >= 0) {
      if (jj == 0) mfma_first<F16>(st.acc[c][T], st.A[e >= 0 ? e : 0], st.B[F % (PD + 1)]);
      else mfma_acc<F16>(st.acc[c][T], st.A[e >= 0 ? e : 0], st.B[F % (PD + 1)]);
    }
  }
  constexpr int T1 = tile_ending_at(SC, F - 3), T2 = tile_ending_at(SC, F - 4), T3 = tile_ending_at(SC, F - 5);
  if constexpr (T1 >= 0) mrg1<(T1 >= 0 ? T1 : 0)>(st);
  if constexpr (SC > 1 && T1 == g.t0) {
    wait_cnt(cx + 28 + 4 * SC, 1);
    st.sv = *(volatile LDS_AS float*)(cx + 16 * (SC - 2) + st.rowoff);
  }
  if constexpr (T2 >= 0) mrg2(st);
  if constexpr (T3 >= 0) {
    if constexpr (STREAMED && T3 >= 4) wait_cnt(cy + 32 + 4 * (T3 - 4), 4);
    mrg3<512 * (STREAMED ? (T3 & 3) : T3), F16>(st, yp, oscale);
    signal_nb(cy + 4 * T3);
  }
  __builtin_amdgcn_sched_barrier(0);
  if constexpr (F + 1 < nstep(SC)) conv_step<SC, (F + 1 < nstep(SC) ? F + 1 : 0)>(st, xb, ab, yp, cx, cy, oscale);
}

__device__ __forceinline__ f32x2 cmul(f32x2 a, f32x2 b) {
  f32x2 r;
  asm("v_pk_mul_f32 %0, %1, %2 op_sel:[0,0] op_sel_hi:[0,1]\n\t"
      "v_pk_fma_f32 %0, %1, %2, %0 op_sel:[1,1,0] op_sel_hi:[1,0,1] neg_lo:[1,0,0]" : "=&v"(r) : "v"(a), "v"(b));
  return r;
}
__device__ __forceinline__ f32x2 cpow(float al, float rh, float rl, float m) {
  const float e = __builtin_amdgcn_exp2f(al * m);
  const float f = __builtin_fmaf(rl, m, __builtin_amdgcn_fractf(rh * m));
  f32x2 z = {e * __builtin_amdgcn_cosf(f), e * __builtin_amdgcn_sinf(f)};
  return z;
}
__device__ __forceinline__ void pow2_scale(u32 mbits, int T, float& s, float& inv) {
  int e = (int)((mbits >> 23) & 0xff);
  e = e < 1 ? 1 : (e > 254 ? 254 : e);
  int se = 254 + T - e;
  se = se < 1 ? 1 : (se > 253 ? 253 : se);
  s = __uint_as_float((u32)se << 23);
  inv = __uint_as_float((u32)(254 - se) << 23);
}

struct Params {
  const u16 *q, *k, *v, *shk, *ssmk, *ssmD, *u, *Are, *Aim, *Bre, *Bim, *Cre, *Cim, *logdt, *Dskip;
  u16* out;
};

template <int SC, int X2>
__device__ __forceinline__ void conv_wave(lptr hs, lptr ys, lptr xs, lptr cx, lptr cy, int lane, float oscale) {
  constexpr int TL = SC == 1 ? 3 : (SC == 2 ? 5 : 7);
  Stream st;
  st.sv = 0.f;
  const int kg = lane >> 4, q = (lane & 15) >> 2, p = lane & 3;
  st.rowoff = 4 * kg;
  lptr ab = hs + (256 - 64 * kg - 8 * q + 8 * p);
  lptr xb = xs + (8 + 16 * kg + 8 * (lane & 15)) + ((lane & 1) ? 0 : X2);
  lptr yp = ys + 8 * lane;
  load_needed<SC, 0, 0, 0>(st, ab); load_needed<SC, 0, 1, 0>(st, ab); load_needed<SC, 0, 2, 0>(st, ab); load_needed<SC, 0, 3, 0>(st, ab);
  if (SC == 0) { wait_cnt(cx, 2); __builtin_amdgcn_s_setprio(2); }
#pragma unroll
  for (int f = 0; f < PDEPTH; ++f) st.B[f] = *(volatile LDS_AS s16x8*)(xb + f * XP);
  conv_step<SC, 0>(st, xb, ab, yp, cx, cy, oscale);
  asm volatile("s_nop 7" : "+v"(st.acc[0][TL]), "+v"(st.acc[1][TL]), "+v"(st.acc[2][TL]), "+v"(st.acc[3][TL]));
  mrg1<TL>(st);
  asm volatile("s_nop 1" : "+v"(st.c0), "+v"(st.c1), "+v"(st.l1), "+v"(st.l2), "+v"(st.l3));
  mrg2(st);
  if (SC == 0) wait_cnt(cy + 44, 4);
  mrg3<512 * (SC == 0 ? 3 : TL), SC != 0>(st, yp, oscale);
  signal_nb(cy + 4 * TL);
  if (SC == 1 || SC == 2) {
    if ((lane & 15) == 0) *(LDS_AS float*)(cx + 16 * (SC - 1) + st.rowoff) = st.sv;
    signal_nb(cx + 32 + 4 * SC);
  }
}

constexpr int RAW_U = 2112;
constexpr int YS_CH = 2080;
constexpr int A_YS = 0;
constexpr int A_RAW = 16640;
constexpr int A_XS = A_RAW + 16 * RAW_U;
constexpr int A_HS = A_XS + XS_ALL;
constexpr int A_CNT = A_HS + 8 * HS_CH;
constexpr int A_END = A_CNT + 128;

__device__ __forceinline__ void head_A(const Params& P, lptr sm, int ch0) {
  const int tid = threadIdx.x, lane = tid & 63;
  const int wave = __builtin_amdgcn_readfirstlane(tid >> 6);
  const u32 lb = (u32)(size_t)sm;
  lptr cx = sm + A_CNT, cy = sm + A_CNT + 64;
  if (tid < 8) *(LDS_AS u32x4*)(sm + A_CNT + tid * 16) = (u32x4){0, 0, 0, 0};
  if (wave < 8) {
    u32 dv;
    asm volatile("global_load_ushort %0, %1, off" : "=v"(dv) : "v"(P.ssmD + ch0 + wave) : "memory");
    const char* hp = (const char*)(P.ssmk + (size_t)(ch0 + wave) * S) + lane * 16;
    dma16h(hp, lb + A_HS + wave * HS_CH + 32);
    dma16h(hp + 1024, lb + A_HS + wave * HS_CH + 32 + 1024);
    BAR();
    const u32x4 z = {0, 0, 0, 0};
    if (lane < 32) *(LDS_AS u32x4*)(sm + A_XS + wave * XS_CH + (lane >> 3) * XP + (lane & 7) * 16) = z;
    *(LDS_AS u32x2*)(sm + A_XS + XS2 + wave * XS_CH + (lane >> 4) * XP + (lane & 15) * 8) = (u32x2){0, 0};
    if (lane < 2) *(LDS_AS u32x4*)(sm + A_HS + wave * HS_CH + lane * 16) = z;
    WAITV(0);
    asm volatile("" : "+v"(dv));
    dma16h(hp + 2048, lb + A_HS + wave * HS_CH + 32 + 2048);
    dma16h(hp + 3072, lb + A_HS + wave * HS_CH + 32 + 3072);
    lptr hs = sm + A_HS + wave * HS_CH;
    if (lane == 0) *(LDS_AS u16*)(hs + 32) = (u16)pk_bf16(bf_lo(*(LDS_AS u16*)(hs + 32)) + bf_lo(dv), 0.f);
    CFENCE();
    conv_wave<0, XS2>(hs, sm + A_YS + wave * YS_CH, sm + A_XS + wave * XS_CH, cx, cy, lane, 1.0f);
  } else {
    __builtin_amdgcn_s_setprio(3);
    {
    const int x = wave - 8;
    const char* kp = (const char*)(P.k + ch0) + (size_t)(x * 64 + lane) * 1280;
    const char* vp = (const char*)(P.v + ch0) + (size_t)(x * 64 + lane) * 1280;
    const u32 rawb = lb + A_RAW + x * 2 * RAW_U;
    lptr raw0 = sm + A_RAW + x * 2 * RAW_U;
#define ROW0(n) (512 * (n))
#define ISSUE(n) do {                                                                                      \
      const u32 slot_ = rawb + ((n) & 1) * RAW_U;                                                          \
      const size_t go_ = (size_t)ROW0(n) * 1280;                                                           \
      if ((n) > 0 || x > 0) { if (lane < 4) dma16(kp + go_ - 4 * 1280, slot_); }                           \
      else if (lane < 4) *(LDS_AS u32x4*)(raw0 + lane * 16) = (u32x4){0, 0, 0, 0};                         \
      dma16(kp + go_, slot_ + 64);                                                                         \
      dma16(vp + go_, slot_ + 1088);                                                                       \
    } while (0)
    u32 wt;
    asm volatile("global_load_ushort %0, %1, off" : "=v"(wt) : "v"(P.shk + (ch0 + (lane & 7)) * 4 + (lane >> 4)) : "memory");
    BAR();
    if (x < EARLYW) { ISSUE(0); WAITV(2); }
    else WAITV(0);
    asm volatile("" : "+v"(wt));
    if (x >= EARLYW) ISSUE(0);
    s16x8 W0, W1;
    {
      const int ch = lane & 7;
      const u32 wv = wt << (16 * (ch & 1));
      u32x4 w = {(ch >> 1) == 0 ? wv : 0u, (ch >> 1) == 1 ? wv : 0u, (ch >> 1) == 2 ? wv : 0u, (ch >> 1) == 3 ? wv : 0u};
      const u32x4 z = {0, 0, 0, 0};
      W0 = __builtin_bit_cast(s16x8, (lane & 8) ? z : w);
      W1 = __builtin_bit_cast(s16x8, (lane & 8) ? w : z);
    }
    const int r = lane & 15, kg = lane >> 4, q = (lane & 15) >> 2, p = lane & 3;
    const int aoff = (8 * (r >> 2) + (r & 3) + 4 - kg) * 16;
    const int voff = 1088 + (8 * (kg + 4 * (p >> 1)) + q) * 16 + 8 * (p & 1);
    lptr dst0 = sm + A_XS + (lane & 7) * XS_CH + 128 + (16 * x + 2 * (kg + 4 * ((lane >> 3) & 1))) * 2;
    const int nblk = 4;
#pragma unroll 1
    for (int n = 0; n < nblk; ++n) {
      WAITV(0);
      lptr raw = raw0 + (n & 1) * RAW_U;
      s16x8 a00 = *(LDS_AS s16x8*)(raw + aoff), a01 = *(LDS_AS s16x8*)(raw + aoff + 512);
      s16x8 a10 = *(LDS_AS s16x8*)(raw + aoff + 64), a11 = *(LDS_AS s16x8*)(raw + aoff + 64 + 512);
      u32x2 V0 = tr_read(raw + voff), V1 = tr_read(raw + voff + 64);
      f32x4 d0 = mfma16<false>(a00, W0, (f32x4){0.f, 0.f, 0.f, 0.f});
      f32x4 d1 = mfma16<false>(a10, W0, (f32x4){0.f, 0.f, 0.f, 0.f});
      d0 = mfma16<false>(a01, W1, d0);
      d1 = mfma16<false>(a11, W1, d1);
      const float v0[4] = {bf_lo(V0.x), bf_hi(V0.x), bf_lo(V0.y), bf_hi(V0.y)};
      const float v1[4] = {bf_lo(V1.x), bf_hi(V1.x), bf_lo(V1.y), bf_hi(V1.y)};
      lptr dst = dst0 + ROW0(n) / 2;
#pragma unroll
      for (int c = 0; c < 4; ++c) {
        float a = d0[c], b = d1[c];
        rbf2(a, b);
        const u32 w = pk_bf16(fmul(a, v0[c]), fmul(b, v1[c]));
        *(LDS_AS u32*)(dst + c * XP) = w;
        *(LDS_AS u32*)(dst + c * XP + XS2) = w;
      }
      signal_nb(cx + 4 * (ROW0(n) / 128 + (x >> 1)));
      if (n + 1 < nblk) ISSUE(n + 1);
    }
#undef ISSUE
#undef ROW0
    }
    const int y = wave & 3, T0 = (wave >> 2) & 1;
    const int frow = 256 * T0 + 4 * (16 * y + (lane & 15)) + (lane >> 4);
    const char* qp = (const char*)(P.q + ch0) + (size_t)frow * 1280;
    char* op = (char*)(P.out + (size_t)frow * 1280 + ch0);
    lptr yb = sm + A_YS + ((lane & 15) >> 2) * YS_CH + 512 * T0 + (lane >> 4) * 128 + y * 32 + 8 * (lane & 3);
    lptr cyt = cy + 4 * T0;
    u32x4 qa, qb, qc, qd;
#define QLOAD(Q, T) asm volatile("global_load_dwordx4 %0, %1, off" : "=v"(Q) : "v"(qp + (size_t)(T) * 256 * 1280) : "memory")
#define FINAL(Q, T) do {                                                                                  \
      u32x2 Y0 = tr_read(yb + 512 * ((T) & 3)), Y1 = tr_read(yb + 512 * ((T) & 3) + 4 * YS_CH);           \
      signal_nb(cyt + 32 + 4 * ((T) & 3));                                                                \
      u32 yv[4] = {Y0.x, Y0.y, Y1.x, Y1.y};                                                               \
      u32x4 o;                                                                                            \
      _Pragma("unroll") for (int e = 0; e < 4; ++e)                                                       \
        o[e] = pk_bf16(fmul(bf_lo(yv[e]), bf_lo(Q[e])), fmul(bf_hi(yv[e]), bf_hi(Q[e])));                 \
      asm volatile("global_store_dwordx4 %0, %1, off\n\ts_nop 1" :: "v"(op + (size_t)(T) * 256 * 2560), "v"(o) : "memory"); \
    } while (0)
    QLOAD(qa, 0);
    QLOAD(qb, 2);
    wait_cnt_slow(cyt + 0, 8, false);
    WAITV(1); asm volatile("" : "+v"(qa));
    FINAL(qa, 0);
    QLOAD(qc, 4);
    wait_cnt_slow(cyt + 8, 8, false);
    WAITV(2); asm volatile("" : "+v"(qb));
    FINAL(qb, 2);
    QLOAD(qd, 6);
    wait_cnt(cyt + 16, 8);
    WAITV(2); asm volatile("" : "+v"(qc));
    FINAL(qc, 4);
    wait_cnt(cyt + 24, 8);
    WAITV(1); asm volatile("" : "+v"(qd));
    FINAL(qd, 6);
#undef QLOAD
#undef FINAL
  }
}

struct Pref { int la, sa, lb, sb, part, nparts; u32 scratch; };
__device__ __forceinline__ void prefetch_all(const Params& P, const Pref& pf, lptr uflag, int lane) {
  const int pa = 8 << pf.sa, pb = 8 << pf.sb;
  const int u0 = UPOS * pa, u1 = u0 + 4 * pb, tot = u1 + (12 - UPOS) * pa;
  bool told = false;
#pragma unroll 1
  for (int wi = pf.part; wi < tot; wi += pf.nparts) {
    const u16* base; int row, line;
    if (wi >= u0 && wi < u1) {
      const int rem = ((wi - u0) << 6) + lane;
      base = P.u; row = rem >> pf.sb; line = pf.lb + (rem & pf.sb);
    } else {
      const int w2 = wi < u0 ? wi : wi - 4 * pb;
      const int e = w2 >> (3 + pf.sa);
      const int rem = ((w2 & (pa - 1)) << 6) + lane;
      const int ty = (0x222102101010ull >> (4 * e)) & 3, dc = (0x321330221100ull >> (4 * e)) & 3;
      base = ty == 0 ? P.k : (ty == 1 ? P.v : P.q);
      row = dc * 512 + (rem >> pf.sa); line = pf.la + (rem & pf.sa);
    }
    if (wi >= u1 - UEARLY * pb && !told) { signal_nb(uflag); told = true; }
    dma4((const char*)base + (size_t)row * 1280 + line * 128, pf.scratch);
    WAITV(0);
  }
  if (!told) signal_nb(uflag);
}

constexpr int B_XS2 = 4 * XS_CH + 8;
constexpr int B_RAW = 0;
constexpr int B_XS = B_RAW + 16384;
constexpr int B_TB = B_XS + 8 * XS_CH + 256;
constexpr int B_TST = 4352;
constexpr int B_HS = B_TB + 8 * B_TST;
constexpr int B_YS = B_HS + 4 * HS_CH;
constexpr int B_KP = B_YS + 4 * HS_CH;
constexpr int B_CNT = B_KP + 4 * 1312;
constexpr int B_CC = B_CNT + 128;
constexpr int B_BX = B_CC + 256;
constexpr int B_END = B_BX + 64;

__device__ __forceinline__ void head_B_main(const Params& P, lptr sm, int ch0, Pref pf) {
  const int tid = threadIdx.x, lane = tid & 63;
  const int wave = __builtin_amdgcn_readfirstlane(tid >> 6);
  const u32 lb = (u32)(size_t)sm;
  lptr cnt = sm + B_CNT, cy = sm + B_CNT + 64;
  if (tid < 28) *(LDS_AS u32x4*)(sm + B_CNT + tid * 16) = (u32x4){0, 0, 0, 0};
  if (wave >= 8) {
    const int s = wave - 8;
    BAR();
    if (s < 4) {
      if (s < NPF) {
        pf.part = s * pf.nparts + pf.part; pf.nparts *= NPF; pf.scratch += s * 256;
        __builtin_amdgcn_s_setprio(3);
        prefetch_all(P, pf, cnt + 24, lane);
        __builtin_amdgcn_s_setprio(0);
      }
      lptr cc = sm + B_CC + s * 64;
      wait_cnt_slow(cc + 44, 1, false);
      wait_cnt(sm + B_BX, 1);
      float us2, uinv2;
      pow2_scale(*(volatile LDS_AS u32*)(cnt + s * 4) << 16, UTGT, us2, uinv2);
      conv_wave<3, B_XS2>(sm + B_HS + s * HS_CH, sm + B_YS + s * HS_CH, sm + B_XS + s * XS_CH, cc, cy, lane, uinv2 * *(volatile LDS_AS float*)(cc + 48));
      return;
    }
    {
      const int y = s - 4;
      wait_cnt_slow(cnt + 24, NPF, false);
      __builtin_amdgcn_s_setprio(3);
      lptr stg = sm + B_RAW + y * 1024;
      const int gq = lane >> 4, q = (lane & 15) >> 2, p = lane & 3;
      lptr rp = stg + (8 * (4 * gq + p) + q) * 8;
      const int ch = lane & 3, ip = lane >> 2;
      float us = 1.f;
#pragma unroll 1
      for (int n = 0; n < 4; ++n) {
        const int blk = 4 * n + y;
        u32x2 r0, r1;
        const char* up = (const char*)(P.u + ch0) + (size_t)(128 * blk + lane) * 1280;
        asm volatile("global_load_dwordx2 %0, %2, off\n\tglobal_load_dwordx2 %1, %3, off\n\ts_waitcnt vmcnt(0)"
                     : "=&v"(r0), "=&v"(r1) : "v"(up), "v"(up + 64 * 1280) : "memory");
        *(LDS_AS u32x2*)(stg + lane * 8) = r0;
        *(LDS_AS u32x2*)(stg + 512 + lane * 8) = r1;
        CFENCE();
        const u32x2 a = tr_read(rp), b = tr_read(rp + 32);
        CFENCE();
        if (n == 0) {
          u16x2 mx = __builtin_elementwise_max(__builtin_bit_cast(u16x2, a.x & 0x7fff7fffu), __builtin_bit_cast(u16x2, a.y & 0x7fff7fffu));
          mx = __builtin_elementwise_max(mx, __builtin_bit_cast(u16x2, b.x & 0x7fff7fffu));
          mx = __builtin_elementwise_max(mx, __builtin_bit_cast(u16x2, b.y & 0x7fff7fffu));
          u32 m1 = max((u32)mx.x, (u32)mx.y);
          m1 = max(m1, (u32)__builtin_amdgcn_update_dpp(0u, m1, 0x124, 0xf, 0xf, false));
          m1 = max(m1, (u32)__builtin_amdgcn_update_dpp(0u, m1, 0x128, 0xf, 0xf, false));
          if ((lane & 15) < 4) __hip_atomic_fetch_max((LDS_AS u32*)(cnt + (lane & 3) * 4), m1, __ATOMIC_RELAXED, __HIP_MEMORY_SCOPE_WORKGROUP);
          signal_nb(cnt + 16);
          wait_cnt(cnt + 16, 4);
          float uinv;
          pow2_scale(*(volatile LDS_AS u32*)(cnt + ch * 4) << 16, UTGT, us, uinv);
        }
        lptr dst = sm + B_XS + ch * XS_CH + 128 + (32 * blk + 2 * ip) * 2;
        u32 w[4] = {pk_f16(fmul(bf_lo(a.x), us), fmul(bf_lo(b.x), us)), pk_f16(fmul(bf_hi(a.x), us), fmul(bf_hi(b.x), us)),
                    pk_f16(fmul(bf_lo(a.y), us), fmul(bf_lo(b.y), us)), pk_f16(fmul(bf_hi(a.y), us), fmul(bf_hi(b.y), us))};
#pragma unroll
        for (int c = 0; c < 4; ++c) {
          *(LDS_AS u32*)(dst + c * XP) = w[c];
          *(LDS_AS u32*)(dst + c * XP + B_XS2) = w[c];
        }
        signal_nb(sm + B_BX + 4 * blk);
        const u32 ov = (((w[0] & 0x7fff7fffu) + 0x04000400u) | ((w[1] & 0x7fff7fffu) + 0x04000400u) |
                        ((w[2] & 0x7fff7fffu) + 0x04000400u) | ((w[3] & 0x7fff7fffu) + 0x04000400u)) & 0x80008000u;
        if (ov) *(volatile LDS_AS u32*)(cnt + 28) = 1u;
      }
    }
    const int y = s - 4;
    const int frow = 4 * (16 * y + (lane & 15)) + (lane >> 4);
    char* op = (char*)(P.out + (size_t)frow * 1280 + 640 + ch0);
    lptr yb = sm + B_YS + ((lane & 15) >> 2) * HS_CH + (lane >> 4) * 128 + y * 32 + 8 * (lane & 3);
#pragma unroll 1
    for (int T = 0; T < 8; ++T) {
      wait_cnt_slow(cy + 4 * T, 4, T == 7);
      u32x2 o = tr_read(yb + 512 * T);
      asm volatile("global_store_dwordx2 %0, %1, off\n\ts_nop 1" :: "v"(op + (size_t)T * 256 * 2560), "v"(o) : "memory");
    }
    return;
  }
  const int w4 = wave & 3, half = wave >> 2;
  const int d = ch0 + w4;
  u32 pr[6];
  u32 cw, ldt;
  {
    const size_t po = (size_t)d * NM * 2 + lane * 4;
    asm volatile("global_load_ushort %0, %1, off" : "=v"(ldt) : "v"(P.logdt + d) : "memory");
    asm volatile("global_load_dword %0, %1, off" : "=v"(pr[0]) : "v"((const char*)P.Are + po) : "memory");
    asm volatile("global_load_dword %0, %1, off" : "=v"(pr[1]) : "v"((const char*)P.Aim + po) : "memory");
    asm volatile("global_load_dword %0, %1, off" : "=v"(pr[2]) : "v"((const char*)P.Bre + po) : "memory");
    asm volatile("global_load_dword %0, %1, off" : "=v"(pr[3]) : "v"((const char*)P.Bim + po) : "memory");
    asm volatile("global_load_dword %0, %1, off" : "=v"(pr[4]) : "v"((const char*)P.Cre + po) : "memory");
    asm volatile("global_load_dword %0, %1, off" : "=v"(pr[5]) : "v"((const char*)P.Cim + po) : "memory");
    asm volatile("global_load_ushort %0, %1, off" : "=v"(cw) : "v"(P.Dskip + d) : "memory");
  }
  BAR();
  if (!half) {
    lptr hs = sm + B_HS + wave * HS_CH;
    if (lane < 2) *(LDS_AS u32x4*)(hs + lane * 16) = (u32x4){0, 0, 0, 0};
    if (lane < 32) *(LDS_AS u32x4*)(sm + B_XS + wave * XS_CH + (lane >> 3) * XP + (lane & 7) * 16) = (u32x4){0, 0, 0, 0};
    *(LDS_AS u32x2*)(sm + B_XS + B_XS2 + wave * XS_CH + (lane >> 4) * XP + (lane & 15) * 8) = (u32x2){0, 0};
  }
  WAITV(0);
  asm volatile("" : "+v"(pr[0]), "+v"(pr[1]), "+v"(pr[2]), "+v"(pr[3]), "+v"(pr[4]), "+v"(pr[5]), "+v"(ldt), "+v"(cw));
  lptr tb = sm + B_TB + wave * B_TST;
  constexpr int RS = 272;
  const float dt = rbf(expf(bf_lo(ldt)));
  float cs, cinv;
  f32x2 G[8];
  float al, rh, rl;
  {
    const float aa = dt * (half ? bf_hi(pr[0]) : bf_lo(pr[0]));
    const float bb = dt * (half ? bf_hi(pr[1]) : bf_lo(pr[1]));
    al = aa * 1.44269504088896341f;
    const float I2PH = 0.15915494309189535f, I2PL = 6.4206383e-09f;
    rh = bb * I2PH;
    rl = __builtin_fmaf(bb, I2PL, __builtin_fmaf(bb, I2PH, -rh));
  }
  lptr tw = tb + lane * 4;
  lptr trd = tb + (lane & 15) * RS + (lane >> 4) * 16;
  {
    const f32x2 z8 = cpow(al, -rh, -rl, 8.f);
    const f32x2 z16 = cmul(z8, z8);
    f32x2 fo = z8, fe = z16;
    *(LDS_AS u32*)(tw) = pk_f16(1.f, 0.f);
#pragma unroll
    for (int j = 1; j < 16; j += 2) {
      *(LDS_AS u32*)(tw + j * RS) = pk_f16v(fo);
      if (j < 15) {
        *(LDS_AS u32*)(tw + (j + 1) * RS) = pk_f16v(fe);
        fo = cmul(fo, z16);
        if (j < 13) fe = cmul(fe, z16);
      }
    }
  }
  {
    f32x2 c0 = cmul((f32x2){bf_lo(pr[4]), bf_lo(pr[5])}, (f32x2){dt * bf_lo(pr[2]), dt * bf_lo(pr[3])});
    f32x2 c1 = cmul((f32x2){bf_hi(pr[4]), bf_hi(pr[5])}, (f32x2){dt * bf_hi(pr[2]), dt * bf_hi(pr[3])});
    float sb = fabsf(c0.x) + fabsf(c0.y) + fabsf(c1.x) + fabsf(c1.y);
#pragma unroll
    for (int o = 32; o >= 1; o >>= 1) sb += __shfl_xor(sb, o);
    sb += fabsf(bf_lo(cw));
    pow2_scale(__float_as_uint(sb), 14, cs, cinv);
    G[0] = half ? c1 : c0;
    G[0] *= cs;
  }
  CFENCE();
  f16x8 Ff[4];
#pragma unroll
  for (int ks = 0; ks < 4; ++ks) Ff[ks] = *(LDS_AS f16x8*)(trd + ks * 64);
  CFENCE();
  f32x4 kacc[3];
  const f32x2 v1 = cpow(al, rh, rl, 128.f);
  {
    const f32x2 z1 = cpow(al, rh, rl, 1.f);
    const f32x2 z2 = cmul(z1, z1);
    G[1] = cmul(G[0], z1);
#pragma unroll
    for (int p = 2; p < 8; ++p) G[p] = cmul(G[p - 2], z2);
#pragma unroll
    for (int p = 0; p < 8; ++p) {
      *(LDS_AS u32*)(tw + p * RS) = pk_f16v(G[p]);
      *(LDS_AS u32*)(tw + (8 + p) * RS) = pk_f16v(cmul(G[p], v1));
    }
    CFENCE();
    kacc[0] = (f32x4){0.f, 0.f, 0.f, 0.f};
#pragma unroll
    for (int ks = 0; ks < 4; ++ks)
      kacc[0] = __builtin_amdgcn_mfma_f32_16x16x32_f16(*(LDS_AS f16x8*)(trd + ks * 64), Ff[ks], kacc[0], 0, 0, 0);
    CFENCE();
  }
  const f32x2 v2 = cmul(v1, v1);
  const f32x2 v4 = cpow(al, rh, rl, 512.f);
  {
#pragma unroll
    for (int p = 0; p < 4; ++p) {
      f32x2 t = cmul(G[2 * p], v2);
      *(LDS_AS u32*)(tw + p * RS) = pk_f16v(t);
      *(LDS_AS u32*)(tw + (4 + p) * RS) = pk_f16v(cmul(t, v1));
    }
#pragma unroll
    for (int p = 0; p < 2; ++p) {
      f32x2 t = cmul(G[4 * p], v4);
#pragma unroll
      for (int a = 0; a < 4; ++a) {
        *(LDS_AS u32*)(tw + (8 + 2 * a + p) * RS) = pk_f16v(t);
        if (a < 3) t = cmul(t, v1);
      }
    }
    CFENCE();
    kacc[1] = (f32x4){0.f, 0.f, 0.f, 0.f};
#pragma unroll
    for (int ks = 0; ks < 4; ++ks)
      kacc[1] = __builtin_amdgcn_mfma_f32_16x16x32_f16(*(LDS_AS f16x8*)(trd + ks * 64), Ff[ks], kacc[1], 0, 0, 0);
    CFENCE();
  }
  {
    f32x2 te = cmul(G[0], cmul(v4, v4));
    f32x2 to = cmul(te, v1);
#pragma unroll
    for (int a = 0; a < 9; a += 2) {
      *(LDS_AS u32*)(tw + a * RS) = pk_f16v(te);
      if (a < 8) {
        *(LDS_AS u32*)(tw + (a + 1) * RS) = pk_f16v(to);
        te = cmul(te, v2);
        if (a < 6) to = cmul(to, v2);
      }
    }
    CFENCE();
    kacc[2] = (f32x4){0.f, 0.f, 0.f, 0.f};
#pragma unroll
    for (int ks = 0; ks < 4; ++ks)
      kacc[2] = __builtin_amdgcn_mfma_f32_16x16x32_f16(*(LDS_AS f16x8*)(trd + ks * 64), Ff[ks], kacc[2], 0, 0, 0);
    CFENCE();
  }
  if (half) {
#pragma unroll
    for (int t = 0; t < 3; ++t) *(LDS_AS f32x4*)(tb + t * 1024 + lane * 16) = kacc[t];
    signal_nb(cnt + 48 + 4 * w4);
    lptr cc = sm + B_CC + w4 * 64;
    wait_cnt_slow(cc + 44, 1, false);
    wait_cnt(sm + B_BX, 1);
    float us2, uinv2;
    pow2_scale(*(volatile LDS_AS u32*)(cnt + w4 * 4) << 16, UTGT, us2, uinv2);
    __builtin_amdgcn_s_setprio(1);
    conv_wave<2, B_XS2>(sm + B_HS + w4 * HS_CH, sm + B_YS + w4 * HS_CH, sm + B_XS + w4 * XS_CH, cc, cy, lane, uinv2 * cinv);
    return;
  }
  wait_cnt(cnt + 48 + 4 * w4, 1);
  lptr Q = sm + B_KP + wave * 1312;
  lptr hs = sm + B_HS + wave * HS_CH;
  {
    lptr ob = sm + B_TB + (wave + 4) * B_TST;
    const int r16 = lane & 15, kg = lane >> 4;
#pragma unroll
    for (int t = 0; t < 3; ++t) {
      f32x4 o = *(LDS_AS f32x4*)(ob + t * 1024 + lane * 16);
      o += kacc[t];
      if (t == 0 && lane == 0) o[0] += bf_lo(cw) * cs;
      u32 a = pk_f16(o[0], o[1]), b = pk_f16(o[2], o[3]);
      if (t == 0) {
        u32x2 w = {a, b};
        *(LDS_AS u32x2*)(hs + 32 + 2 * (4 * (kg & 1) + 128 * (kg >> 1) + 8 * r16)) = w;
      } else if (t == 1) {
        if (kg < 2) {
          u32x2 w = {a, b};
          *(LDS_AS u32x2*)(Q + 2 * (64 * kg + 4 * r16)) = w;
        } else {
          *(LDS_AS u32*)(Q + 2 * (128 + 64 * (kg - 2) + 2 * r16)) = a;
          *(LDS_AS u32*)(Q + 2 * (128 + 64 * (kg - 2) + 32 + 2 * r16)) = b;
        }
      } else {
        if (kg < 2) {
          *(LDS_AS u16*)(Q + 2 * (256 + 64 * kg + r16)) = (u16)a;
          *(LDS_AS u16*)(Q + 2 * (256 + 64 * kg + 16 + r16)) = (u16)(a >> 16);
          *(LDS_AS u16*)(Q + 2 * (256 + 64 * kg + 32 + r16)) = (u16)b;
          *(LDS_AS u16*)(Q + 2 * (256 + 64 * kg + 48 + r16)) = (u16)(b >> 16);
        } else if (lane == 32) {
          *(LDS_AS u16*)(Q + 2 * 384) = (u16)a;
        }
      }
    }
  }
  CFENCE();
  {
    {
      u32 w = *(LDS_AS u32*)(Q + 2 * (2 * lane));
      u32 v2q = *(LDS_AS u16*)(Q + 2 * (2 * lane + 2));
      u32x2 o = {__builtin_amdgcn_perm(w, w, 0x01000100), __builtin_amdgcn_perm(v2q, w, 0x05040302)};
      *(LDS_AS u32x2*)(hs + 32 + 512 + lane * 8) = o;
    }
    {
      u32 w = *(LDS_AS u32*)(Q + 2 * (128 + 2 * lane));
      u32 v2q = *(LDS_AS u16*)(Q + 2 * (128 + 2 * lane + 2));
      u32x4 o = {__builtin_amdgcn_perm(w, w, 0x01000100), w, __builtin_amdgcn_perm(w, w, 0x03020302), __builtin_amdgcn_perm(v2q, v2q, 0x01000100)};
      *(LDS_AS u32x4*)(hs + 32 + 1024 + lane * 16) = o;
    }
    {
      u32 w = *(LDS_AS u32*)(Q + 2 * (256 + 2 * lane));
      u32 v2q = *(LDS_AS u16*)(Q + 2 * (256 + 2 * lane + 2));
      u32 a0 = __builtin_amdgcn_perm(w, w, 0x01000100), a1 = __builtin_amdgcn_perm(w, w, 0x03020302), a2 = __builtin_amdgcn_perm(v2q, v2q, 0x01000100);
      u32x4 o0 = {a0, a0, w, a1}, o1 = {a1, a1, a2, a2};
      *(LDS_AS u32x4*)(hs + 32 + 2048 + lane * 32) = o0;
      *(LDS_AS u32x4*)(hs + 32 + 2048 + lane * 32 + 16) = o1;
    }
  }
  lptr cc = sm + B_CC + wave * 64;
  if (lane == 0) *(LDS_AS float*)(cc + 48) = cinv;
  signal_nb(cc + 44);
  wait_cnt(sm + B_BX, 1);
  float us2, uinv2;
  pow2_scale(*(volatile LDS_AS u32*)(cnt + wave * 4) << 16, UTGT, us2, uinv2);
  __builtin_amdgcn_s_setprio(2);
  conv_wave<1, B_XS2>(hs, sm + B_YS + wave * HS_CH, sm + B_XS + wave * XS_CH, cc, cy, lane, uinv2 * cinv);
}

__device__ __forceinline__ void head_B(const Params& P, lptr sm, int ch0, Pref pf) {
  head_B_main(P, sm, ch0, pf);
  BAR();
  if (__builtin_amdgcn_readfirstlane(*(volatile LDS_AS u32*)(sm + B_CNT + 28)) == 0u) return;
  WAITV(0);
  const int tid = threadIdx.x;
  LDS_AS float* uf = (LDS_AS float*)(sm + B_XS);
  for (int i = tid; i < 4 * S; i += 1024) uf[(i & 3) * S + (i >> 2)] = bf_lo(P.u[(size_t)(i >> 2) * D + ch0 + (i & 3)]);
  BAR();
  for (int i = tid; i < 4 * S; i += 1024) {
    const int c = i >> 11, t = i & (S - 1);
    const LDS_AS _Float16* kf = (const LDS_AS _Float16*)(sm + B_HS + c * HS_CH + 32);
    float acc = 0.f;
    for (int l = 0; l <= t; ++l) acc = __builtin_fmaf((float)kf[l], uf[c * S + t - l], acc);
    acc *= *(LDS_AS float*)(sm + B_CC + c * 64 + 48);
    P.out[(size_t)t * 1280 + 640 + ch0 + c] = (u16)pk_bf16(acc, 0.f);
  }
}

__global__ __launch_bounds__(1024) void k_fused(Params P) {
  constexpr int SCRATCH = A_END > B_END ? A_END : B_END;
  __shared__ __attribute__((aligned(2048))) char smem[SCRATCH + 2048];
  lptr sm = (lptr)smem;
  const int b = blockIdx.x;
  const int xcd = b & 7, slot = b >> 3;
  const int nA = xcd < 4 ? 12 : 8, nB = xcd < 4 ? 16 : 24;
  const int a0 = xcd < 4 ? 96 * xcd : 384 + 64 * (xcd - 4);
  const int b0 = xcd < 4 ? 64 * xcd : 256 + 96 * (xcd - 4);
  Pref pf;
  pf.la = (a0 * 2) >> 7; pf.sa = (((a0 + nA * 8) * 2 - 1) >> 7) - pf.la;
  pf.lb = (b0 * 2) >> 7; pf.sb = (((b0 + nB * 4) * 2 - 1) >> 7) - pf.lb;
  pf.part = slot - nA; pf.nparts = nB; pf.scratch = (u32)(size_t)sm + SCRATCH;
  if (slot < nA) {
    head_A(P, sm, a0 + slot * 8);
  } else if (slot < nA + nB) {
    head_B(P, sm, b0 + (slot - nA) * 4, pf);
  }
}

extern "C" void kernel_launch(void* const* d_in, const int* in_sizes, int n_in,
                              void* d_out, int out_size, void* d_ws, size_t ws_size,
                              hipStream_t stream) {
  Params P;
  P.q = (const u16*)d_in[0]; P.k = (const u16*)d_in[1]; P.v = (const u16*)d_in[2];
  P.shk = (const u16*)d_in[3]; P.ssmk = (const u16*)d_in[4]; P.ssmD = (const u16*)d_in[5];
  P.u = (const u16*)d_in[6]; P.Are = (const u16*)d_in[7]; P.Aim = (const u16*)d_in[8];
  P.Bre = (const u16*)d_in[9]; P.Bim = (const u16*)d_in[10]; P.Cre = (const u16*)d_in[11];
  P.Cim = (const u16*)d_in[12]; P.logdt = (const u16*)d_in[13]; P.Dskip = (const u16*)d_in[14];
  P.out = (u16*)d_out;
  k_fused<<<256, 1024, 0, stream>>>(P);
}
```
